```python
import torch

def _softmax(x, axis=-1):
    return torch.softmax(x, dim=axis)

def _gelu(x):
    return torch.nn.functional.gelu(x, approximate='tanh')

def _silu(x):
    return torch.nn.functional.silu(x)

def _sigmoid(x):
    return torch.sigmoid(x)

def _layernorm(x, axis=-1, eps=1e-05):
    m = x.mean(dim=axis, keepdim=True)
    v = x.var(dim=axis, keepdim=True, unbiased=False)
    return (x - m) / torch.sqrt(v + eps)

def _scatter_add(tgt, idx, src, axis=0):
    return tgt.clone().index_add_(axis, idx, src)

def _segment_sum(src, seg, n):
    out = torch.zeros((n,) + src.shape[1:], dtype=src.dtype)
    return out.index_add_(0, seg, src)

def _pad(x, pw):
    flat = tuple((v for pair in reversed(pw) for v in pair))
    return torch.nn.functional.pad(x, flat)
kernel_name = 'adaLN_cond__sliding_local_dilated__glu_gated_geglu__prompt'

def setup_inputs():

    _w = lambda a: (lambda t: t.to(torch.bfloat16) if t.is_floating_point() else t)(torch.as_tensor(a))

    def _A_setup():
        B, S, D = (1, 2048, 512)
        return {'x': _w((lambda *s: torch.randn(*s, dtype=torch.bfloat16) * 0.05)(B, S, D)), 'wqkv': _w((lambda *s: torch.randn(*s, dtype=torch.bfloat16) * 0.05)(D, 3 * D)), 'wo': _w((lambda *s: torch.randn(*s, dtype=torch.bfloat16) * 0.05)(D, D))}
    _A_in = _A_setup()

    def _B_setup():
        B, S, Dm, Df = (1, 2048, 512, 1792)
        return {'x': _w((lambda *s: torch.randn(*s, dtype=torch.bfloat16) * 0.05)(B, S, Dm)), 'w_gate': _w((lambda *s: torch.randn(*s, dtype=torch.bfloat16) * 0.8)(Dm, Df) * Dm ** (-0.5)), 'w_up': _w((lambda *s: torch.randn(*s, dtype=torch.bfloat16) * 0.8)(Dm, Df) * Dm ** (-0.5)), 'w_down': _w((lambda *s: torch.randn(*s, dtype=torch.bfloat16) * 0.8)(Df, Dm) * Df ** (-0.5))}
    _B_in = _B_setup()
    return {'A_x': _A_in['x'], 'A_wqkv': _A_in['wqkv'], 'A_wo': _A_in['wo'], 'B_x': _B_in['x'], 'B_w_gate': _B_in['w_gate'], 'B_w_up': _B_in['w_up'], 'B_w_down': _B_in['w_down'], 'c': _w((lambda *s: torch.randn(*s, dtype=torch.bfloat16) * 0.2)(1, 512) * 8.0), 'w_mod': _w((lambda *s: torch.randn(*s, dtype=torch.bfloat16) * 0.2)(512, 6 * 512) * 512 ** (-0.5) * 8.0)}

def reference(A_x, A_wqkv, A_wo, B_x, B_w_gate, B_w_up, B_w_down, c, w_mod):

    def _A_ref(x, wqkv, wo):
        B, S, D = x.shape
        H = 32
        Dh = D // H
        W, dil = (8, 2)
        qkv = (lambda x, *d: x.permute(*d))((x @ wqkv).reshape(B, S, 3, H, Dh), 2, 0, 3, 1, 4)
        q, k, v = (qkv[0], qkv[1], qkv[2])
        scale = Dh ** (-0.5)
        pos = torch.arange(S)
        off = torch.arange(W) * dil
        idx = pos[:, None] - off[None, :]
        mask = idx >= 0
        idx_c = torch.where(mask, idx, 0).reshape(-1)
        kg = k[:, :, idx_c, :].reshape(B, H, S, W, Dh)
        vg = v[:, :, idx_c, :].reshape(B, H, S, W, Dh)
        logits = torch.einsum('bhsd,bhswd->bhsw', q, kg) * scale
        logits = torch.where(mask[None, None, :, :], logits, -1000000000.0)
        attn = _softmax(logits, axis=-1)
        o = torch.einsum('bhsw,bhswd->bhsd', attn, vg)
        return (lambda x, *d: x.permute(*d))(o, 0, 2, 1, 3).reshape(B, S, D) @ wo

    def _B_ref(x, w_gate, w_up, w_down):
        g = torch.einsum('bsd,df->bsf', x, w_gate)
        u = torch.einsum('bsd,df->bsf', x, w_up)
        h = _gelu(g) * u
        return torch.einsum('bsf,fd->bsd', h, w_down)
    x = A_x
    mod = (_silu(c) @ w_mod).reshape(c.shape[0], 6, 1, -1)
    sa, ca, ga, sb, cb, gb = (mod[:, 0].clone(), mod[:, 1].clone(), mod[:, 2].clone(), mod[:, 3].clone(), mod[:, 4].clone(), mod[:, 5].clone())
    h = _layernorm(x, axis=-1) * (1 + ca) + sa
    x = x + ga * _A_ref(h, A_wqkv, A_wo)
    h = _layernorm(x, axis=-1) * (1 + cb) + sb
    x = x + gb * _B_ref(h, B_w_gate, B_w_up, B_w_down)
    return x
```

```cpp
#include <hip/hip_runtime.h>
#include <hip/hip_bf16.h>
#include <stdint.h>


typedef unsigned short u16;
typedef short bf16x8 __attribute__((ext_vector_type(8)));
typedef short bf16x4 __attribute__((ext_vector_type(4)));
typedef float f32x4 __attribute__((ext_vector_type(4)));
typedef unsigned u32x4 __attribute__((ext_vector_type(4)));
typedef unsigned u32x2 __attribute__((ext_vector_type(2)));
typedef __attribute__((address_space(3))) bf16x4 lds_bf16x4;

constexpr int S_ = 2048, D_ = 512, DF_ = 1792;

__device__ __forceinline__ float bf2f(u16 v) { return __uint_as_float(((unsigned)v) << 16); }
__device__ __forceinline__ u16 f2bf(float f) { __bf16 b = (__bf16)f; return __builtin_bit_cast(u16, b); }
__device__ __forceinline__ float rbf(float f) { return bf2f(f2bf(f)); }
__device__ __forceinline__ float lo16(unsigned u) { return __uint_as_float(u << 16); }
__device__ __forceinline__ float hi16(unsigned u) { return __uint_as_float(u & 0xffff0000u); }
__device__ __forceinline__ unsigned pack2(float a, float b) { return (unsigned)f2bf(a) | ((unsigned)f2bf(b) << 16); }

__device__ __forceinline__ bf16x4 tr_read(const u16* p) {
  return __builtin_amdgcn_ds_read_tr16_b64_v4i16((lds_bf16x4*)p);
}

constexpr int PREP_MOD_BLOCKS = 96;
__global__ __launch_bounds__(256) void prep_kernel(const u16* __restrict__ x, const u16* __restrict__ c,
                                                   const u16* __restrict__ wmod, float* __restrict__ mod,
                                                   u16* __restrict__ xn) {
  __shared__ float red[64][33];
  const int tid = threadIdx.x;
  if (blockIdx.x < PREP_MOD_BLOCKS) {
    const int n0 = blockIdx.x * 32;
    const int cg = tid & 3, kl = tid >> 2;
    float acc[8];
#pragma unroll
    for (int j = 0; j < 8; ++j) acc[j] = 0.f;
#pragma unroll
    for (int i = 0; i < 8; ++i) {
      const int k = kl + 64 * i;
      const float cv = bf2f(c[k]);
      const float sv = rbf(cv / (1.f + __expf(-cv)));
      const u32x4 w = *(const u32x4*)(wmod + (size_t)k * 3072 + n0 + cg * 8);
#pragma unroll
      for (int j = 0; j < 4; ++j) {
        acc[2 * j] += sv * lo16(w[j]);
        acc[2 * j + 1] += sv * hi16(w[j]);
      }
    }
#pragma unroll
    for (int j = 0; j < 8; ++j) red[kl][cg * 8 + j] = acc[j];
    __syncthreads();
    if (tid < 32) {
      float s = 0.f;
#pragma unroll 8
      for (int i = 0; i < 64; ++i) s += red[i][tid];
      mod[n0 + tid] = rbf(s);
    }
  } else {
    const int b = blockIdx.x - PREP_MOD_BLOCKS;
    const int wave = tid >> 6, lane = tid & 63;
#pragma unroll
    for (int rr = 0; rr < 2; ++rr) {
      const int row = b * 8 + wave * 2 + rr;
      const u32x4 v = *(const u32x4*)(x + (size_t)row * D_ + lane * 8);
      float f[8];
#pragma unroll
      for (int j = 0; j < 4; ++j) { f[2 * j] = lo16(v[j]); f[2 * j + 1] = hi16(v[j]); }
      float s = 0.f;
#pragma unroll
      for (int j = 0; j < 8; ++j) s += f[j];
#pragma unroll
      for (int o = 32; o >= 1; o >>= 1) s += __shfl_xor(s, o);
      const float mean = s * (1.f / D_);
      float q = 0.f;
#pragma unroll
      for (int j = 0; j < 8; ++j) { const float d = f[j] - mean; q += d * d; }
#pragma unroll
      for (int o = 32; o >= 1; o >>= 1) q += __shfl_xor(q, o);
      const float var = rbf(q * (1.f / D_));
      const float mb = rbf(mean);
      const float den = rbf(sqrtf(rbf(var + 1e-5f)));
      u32x4 o;
#pragma unroll
      for (int j = 0; j < 4; ++j) o[j] = pack2(rbf(f[2 * j] - mb) / den, rbf(f[2 * j + 1] - mb) / den);
      *(u32x4*)(xn + (size_t)row * D_ + lane * 8) = o;
    }
  }
}

template <int BM, int BN, int WM, int WN>
struct Core {
  static constexpr int BK = 64;
  static constexpr int NT = WM * WN * 64;
  static constexpr int LDA = BK + 8;
  static constexpr int LDB = BN + 16;
  static constexpr int A_ELEMS = BM * LDA, B_ELEMS = BK * LDB;
  static constexpr int STAGE_ELEMS = A_ELEMS + B_ELEMS;
  static constexpr int ACH = BM * (BK / 8), BCH = BK * (BN / 8);
  static constexpr int AIT = (ACH + NT - 1) / NT, BIT = (BCH + NT - 1) / NT;
  static constexpr int MT = BM / WM / 16, NTL = BN / WN / 16;
};

template <int BM, int BN, int WM, int WN>
__device__ __forceinline__ void mma_step(const u16* __restrict__ As, const u16* __restrict__ Bs,
                                         f32x4 (&acc)[Core<BM, BN, WM, WN>::MT][Core<BM, BN, WM, WN>::NTL],
                                         int wm, int wn, int lane) {
  using C = Core<BM, BN, WM, WN>;
  const int g = lane >> 4, q = (lane & 15) >> 2, p = lane & 3;
#pragma unroll
  for (int ks = 0; ks < 2; ++ks) {
    bf16x8 a[C::MT], w[C::NTL];
#pragma unroll
    for (int mt = 0; mt < C::MT; ++mt)
      a[mt] = *(const bf16x8*)(As + (wm * (BM / WM) + mt * 16 + (lane & 15)) * C::LDA + ks * 32 + g * 8);
#pragma unroll
    for (int nt = 0; nt < C::NTL; ++nt) {
      const u16* bp = Bs + (ks * 32 + 8 * g + q) * C::LDB + wn * (BN / WN) + nt * 16 + 4 * p;
      const bf16x4 t0 = tr_read(bp), t1 = tr_read(bp + 4 * C::LDB);
      w[nt] = __builtin_shufflevector(t0, t1, 0, 1, 2, 3, 4, 5, 6, 7);
    }
#pragma unroll
    for (int mt = 0; mt < C::MT; ++mt)
#pragma unroll
      for (int nt = 0; nt < C::NTL; ++nt)
        acc[mt][nt] = __builtin_amdgcn_mfma_f32_16x16x32_bf16(w[nt], a[mt], acc[mt][nt], 0, 0, 0);
  }
}

__global__ __launch_bounds__(256) void qkv_attn_kernel(const u16* __restrict__ xn, const u16* __restrict__ wqkv,
                                                       const float* __restrict__ mod, u16* __restrict__ o) {
  using C = Core<80, 192, 1, 4>;
  constexpr int TLD = 200;
  constexpr int SMEM_ELEMS = (2 * C::STAGE_ELEMS > 80 * TLD) ? 2 * C::STAGE_ELEMS : 80 * TLD;
  __shared__ __attribute__((aligned(16))) u16 smem[SMEM_ELEMS];
  __shared__ float s_opc[D_], s_sa[D_];
  const int tid = threadIdx.x, lane = tid & 63, wn = tid >> 6;
  const int rb = blockIdx.x >> 3, hg = blockIdx.x & 7;
  const int m0 = rb * 64;
  for (int i = tid; i < D_; i += 256) { s_sa[i] = mod[i]; s_opc[i] = rbf(1.f + mod[D_ + i]); }

  u32x4 ra[C::AIT], rbv[C::BIT];
  auto gload = [&](int k0) {
#pragma unroll
    for (int i = 0; i < C::AIT; ++i) {
      const int ch = tid + i * 256;
      if (ch < C::ACH) {
        const int r = ch >> 3, kc = ch & 7;
        int grow = m0 - 16 + r; grow = grow < 0 ? 0 : grow;
        ra[i] = *(const u32x4*)(xn + (size_t)grow * D_ + k0 + kc * 8);
      }
    }
#pragma unroll
    for (int i = 0; i < C::BIT; ++i) {
      const int ch = tid + i * 256;
      const int kr = ch / 24, nc = ch % 24;
      const int seg = nc >> 3, c8 = nc & 7;
      rbv[i] = *(const u32x4*)(wqkv + (size_t)(k0 + kr) * 1536 + seg * 512 + hg * 64 + c8 * 8);
    }
  };
  auto swrite = [&](u16* st, int k0) {
    u16* As = st; u16* Bs = st + C::A_ELEMS;
#pragma unroll
    for (int i = 0; i < C::AIT; ++i) {
      const int ch = tid + i * 256;
      if (ch < C::ACH) {
        const int r = ch >> 3, kc = ch & 7;
        u32x4 w;
#pragma unroll
        for (int j = 0; j < 4; ++j) {
          const int k = k0 + kc * 8 + 2 * j;
          const float a0 = rbf(rbf(lo16(ra[i][j]) * s_opc[k]) + s_sa[k]);
          const float a1 = rbf(rbf(hi16(ra[i][j]) * s_opc[k + 1]) + s_sa[k + 1]);
          w[j] = pack2(a0, a1);
        }
        *(u32x4*)(As + r * C::LDA + kc * 8) = w;
      }
    }
#pragma unroll
    for (int i = 0; i < C::BIT; ++i) {
      const int ch = tid + i * 256;
      const int kr = ch / 24, nc = ch % 24;
      *(u32x4*)(Bs + kr * C::LDB + nc * 8) = rbv[i];
    }
  };

  f32x4 acc[C::MT][C::NTL];
#pragma unroll
  for (int a = 0; a < C::MT; ++a)
#pragma unroll
    for (int b = 0; b < C::NTL; ++b) acc[a][b] = (f32x4){0.f, 0.f, 0.f, 0.f};

  gload(0);
  __syncthreads();
  swrite(smem, 0);
  __syncthreads();
  constexpr int NK = D_ / 64;
  for (int kt = 0; kt < NK; ++kt) {
    if (kt + 1 < NK) gload((kt + 1) * 64);
    const u16* st = smem + (kt & 1) * C::STAGE_ELEMS;
    mma_step<80, 192, 1, 4>(st, st + C::A_ELEMS, acc, 0, wn, lane);
    if (kt + 1 < NK) swrite(smem + ((kt + 1) & 1) * C::STAGE_ELEMS, (kt + 1) * 64);
    __syncthreads();
  }
#pragma unroll
  for (int mt = 0; mt < C::MT; ++mt)
#pragma unroll
    for (int nt = 0; nt < C::NTL; ++nt) {
      const int m = mt * 16 + (lane & 15), n = wn * 48 + nt * 16 + (lane >> 4) * 4;
      u32x2 w;
      w[0] = pack2(acc[mt][nt][0], acc[mt][nt][1]);
      w[1] = pack2(acc[mt][nt][2], acc[mt][nt][3]);
      *(u32x2*)(smem + m * TLD + n) = w;
    }
  __syncthreads();
  {
    const int r = tid >> 2, hl = tid & 3;
    const u16* qp = smem + (16 + r) * TLD + hl * 16;
    float q[16];
    {
      const u32x4 v0 = *(const u32x4*)qp, v1 = *(const u32x4*)(qp + 8);
#pragma unroll
      for (int j = 0; j < 4; ++j) { q[2 * j] = lo16(v0[j]); q[2 * j + 1] = hi16(v0[j]); q[8 + 2 * j] = lo16(v1[j]); q[9 + 2 * j] = hi16(v1[j]); }
    }
    float lg[8];
    float mx = -3.0e38f;
#pragma unroll
    for (int w = 0; w < 8; ++w) {
      const int row = 16 + r - 2 * w;
      const u16* kp = smem + row * TLD + 64 + hl * 16;
      const u32x4 v0 = *(const u32x4*)kp, v1 = *(const u32x4*)(kp + 8);
      float d = 0.f;
#pragma unroll
      for (int j = 0; j < 4; ++j) {
        d += q[2 * j] * lo16(v0[j]); d += q[2 * j + 1] * hi16(v0[j]);
        d += q[8 + 2 * j] * lo16(v1[j]); d += q[9 + 2 * j] * hi16(v1[j]);
      }
      float l = rbf(rbf(d) * 0.25f);
      if (m0 + r - 2 * w < 0) l = rbf(-1000000000.0f);
      lg[w] = l;
      mx = fmaxf(mx, l);
    }
    float sum = 0.f;
#pragma unroll
    for (int w = 0; w < 8; ++w) { lg[w] = __expf(lg[w] - mx); sum += lg[w]; }
    const float inv = 1.f / sum;
    float ov[16];
#pragma unroll
    for (int j = 0; j < 16; ++j) ov[j] = 0.f;
#pragma unroll
    for (int w = 0; w < 8; ++w) {
      const int row = 16 + r - 2 * w;
      const float pw = rbf(lg[w] * inv);
      const u16* vp = smem + row * TLD + 128 + hl * 16;
      const u32x4 v0 = *(const u32x4*)vp, v1 = *(const u32x4*)(vp + 8);
#pragma unroll
      for (int j = 0; j < 4; ++j) {
        ov[2 * j] += pw * lo16(v0[j]); ov[2 * j + 1] += pw * hi16(v0[j]);
        ov[8 + 2 * j] += pw * lo16(v1[j]); ov[9 + 2 * j] += pw * hi16(v1[j]);
      }
    }
    u32x4 o0, o1;
#pragma unroll
    for (int j = 0; j < 4; ++j) { o0[j] = pack2(ov[2 * j], ov[2 * j + 1]); o1[j] = pack2(ov[8 + 2 * j], ov[9 + 2 * j]); }
    u16* op = o + (size_t)(m0 + r) * D_ + hg * 64 + hl * 16;
    *(u32x4*)op = o0;
    *(u32x4*)(op + 8) = o1;
  }
}

__global__ __launch_bounds__(256) void wo_kernel(const u16* __restrict__ o, const u16* __restrict__ wo,
                                                 const u16* __restrict__ x, const float* __restrict__ mod,
                                                 u16* __restrict__ x1, float2* __restrict__ stats) {
  using C = Core<64, 64, 2, 2>;
  __shared__ __attribute__((aligned(16))) u16 smem[2 * C::STAGE_ELEMS];
  const int tid = threadIdx.x, lane = tid & 63, wid = tid >> 6, wm = wid >> 1, wn = wid & 1;
  const int rb = blockIdx.x >> 3, cb = blockIdx.x & 7;
  const int m0 = rb * 64, n0 = cb * 64;
  u32x4 ra[2], rbv[2];
  auto gload = [&](int k0) {
#pragma unroll
    for (int i = 0; i < 2; ++i) {
      const int ch = tid + i * 256;
      ra[i] = *(const u32x4*)(o + (size_t)(m0 + (ch >> 3)) * D_ + k0 + (ch & 7) * 8);
      rbv[i] = *(const u32x4*)(wo + (size_t)(k0 + (ch >> 3)) * D_ + n0 + (ch & 7) * 8);
    }
  };
  auto swrite = [&](u16* st) {
#pragma unroll
    for (int i = 0; i < 2; ++i) {
      const int ch = tid + i * 256;
      *(u32x4*)(st + (ch >> 3) * C::LDA + (ch & 7) * 8) = ra[i];
      *(u32x4*)(st + C::A_ELEMS + (ch >> 3) * C::LDB + (ch & 7) * 8) = rbv[i];
    }
  };
  f32x4 acc[2][2];
#pragma unroll
  for (int a = 0; a < 2; ++a)
#pragma unroll
    for (int b = 0; b < 2; ++b) acc[a][b] = (f32x4){0.f, 0.f, 0.f, 0.f};
  gload(0);
  swrite(smem);
  __syncthreads();
  constexpr int NK = D_ / 64;
  for (int kt = 0; kt < NK; ++kt) {
    if (kt + 1 < NK) gload((kt + 1) * 64);
    const u16* st = smem + (kt & 1) * C::STAGE_ELEMS;
    mma_step<64, 64, 2, 2>(st, st + C::A_ELEMS, acc, wm, wn, lane);
    if (kt + 1 < NK) swrite(smem + ((kt + 1) & 1) * C::STAGE_ELEMS);
    __syncthreads();
  }
  float* T = (float*)smem;
#pragma unroll
  for (int mt = 0; mt < 2; ++mt)
#pragma unroll
    for (int nt = 0; nt < 2; ++nt) {
      const int m = wm * 32 + mt * 16 + (lane & 15), n = wn * 32 + nt * 16 + (lane >> 4) * 4;
      const u32x2 xv = *(const u32x2*)(x + (size_t)(m0 + m) * D_ + n0 + n);
      float r[4];
      const float4 gav = *(const float4*)(mod + 2 * D_ + n0 + n);
      r[0] = rbf(lo16(xv[0]) + rbf(gav.x * rbf(acc[mt][nt][0])));
      r[1] = rbf(hi16(xv[0]) + rbf(gav.y * rbf(acc[mt][nt][1])));
      r[2] = rbf(lo16(xv[1]) + rbf(gav.z * rbf(acc[mt][nt][2])));
      r[3] = rbf(hi16(xv[1]) + rbf(gav.w * rbf(acc[mt][nt][3])));
      u32x2 w; w[0] = pack2(r[0], r[1]); w[1] = pack2(r[2], r[3]);
      *(u32x2*)(x1 + (size_t)(m0 + m) * D_ + n0 + n) = w;
#pragma unroll
      for (int j = 0; j < 4; ++j) T[m * 65 + n + j] = r[j];
    }
  __syncthreads();
  if (tid < 64) {
    float s = 0.f;
#pragma unroll 16
    for (int j = 0; j < 64; ++j) s += T[tid * 65 + j];
    const float mean = s * (1.f / 64.f);
    float q = 0.f;
#pragma unroll 16
    for (int j = 0; j < 64; ++j) { const float d = T[tid * 65 + j] - mean; q += d * d; }
    stats[(size_t)(m0 + tid) * 8 + cb] = make_float2(mean, q);
  }
}

__global__ __launch_bounds__(512) void gateup_kernel(const u16* __restrict__ x1, const float2* __restrict__ stats,
                                                     const u16* __restrict__ wg, const u16* __restrict__ wu,
                                                     const float* __restrict__ mod, u16* __restrict__ hh) {
  using C = Core<128, 256, 2, 4>;
  __shared__ __attribute__((aligned(16))) u16 smem[2 * C::STAGE_ELEMS];
  __shared__ float s_opc[D_], s_sb[D_], s_mean[128], s_den[128];
  const int tid = threadIdx.x, lane = tid & 63, wid = tid >> 6, wm = wid >> 2, wn = wid & 3;
  const int rb = blockIdx.x / 14, cbk = blockIdx.x % 14;
  const int m0 = rb * 128, f0 = cbk * 128;
  for (int i = tid; i < D_; i += 512) { s_sb[i] = mod[3 * D_ + i]; s_opc[i] = rbf(1.f + mod[4 * D_ + i]); }
  if (tid < 128) {
    const float2* sp = stats + (size_t)(m0 + tid) * 8;
    float mi[8], s = 0.f, q = 0.f;
#pragma unroll
    for (int j = 0; j < 8; ++j) { const float2 v = sp[j]; mi[j] = v.x; s += v.x; q += v.y; }
    const float mean = s * 0.125f;
#pragma unroll
    for (int j = 0; j < 8; ++j) { const float d = mi[j] - mean; q += 64.f * d * d; }
    const float var = rbf(q * (1.f / D_));
    s_mean[tid] = rbf(mean);
    s_den[tid] = rbf(sqrtf(rbf(var + 1e-5f)));
  }
  u32x4 ra[2], rbv[4];
  auto gload = [&](int k0) {
#pragma unroll
    for (int i = 0; i < 2; ++i) {
      const int ch = tid + i * 512;
      ra[i] = *(const u32x4*)(x1 + (size_t)(m0 + (ch >> 3)) * D_ + k0 + (ch & 7) * 8);
    }
#pragma unroll
    for (int i = 0; i < 4; ++i) {
      const int ch = tid + i * 512;
      const int kr = ch >> 5, nc = ch & 31;
      const int w = nc >> 3, isup = (nc >> 2) & 1, c8 = nc & 3;
      const u16* src = isup ? wu : wg;
      rbv[i] = *(const u32x4*)(src + (size_t)(k0 + kr) * DF_ + f0 + w * 32 + c8 * 8);
    }
  };
  auto swrite = [&](u16* st, int k0) {
#pragma unroll
    for (int i = 0; i < 2; ++i) {
      const int ch = tid + i * 512;
      const int r = ch >> 3, kc = ch & 7;
      const float mb = s_mean[r], den = s_den[r];
      u32x4 w;
#pragma unroll
      for (int j = 0; j < 4; ++j) {
        const int k = k0 + kc * 8 + 2 * j;
        const float a0 = rbf(rbf(rbf(rbf(lo16(ra[i][j]) - mb) / den) * s_opc[k]) + s_sb[k]);
        const float a1 = rbf(rbf(rbf(rbf(hi16(ra[i][j]) - mb) / den) * s_opc[k + 1]) + s_sb[k + 1]);
        w[j] = pack2(a0, a1);
      }
      *(u32x4*)(st + r * C::LDA + kc * 8) = w;
    }
#pragma unroll
    for (int i = 0; i < 4; ++i) {
      const int ch = tid + i * 512;
      *(u32x4*)(st + C::A_ELEMS + (ch >> 5) * C::LDB + (ch & 31) * 8) = rbv[i];
    }
  };
  f32x4 acc[C::MT][C::NTL];
#pragma unroll
  for (int a = 0; a < C::MT; ++a)
#pragma unroll
    for (int b = 0; b < C::NTL; ++b) acc[a][b] = (f32x4){0.f, 0.f, 0.f, 0.f};
  gload(0);
  __syncthreads();
  swrite(smem, 0);
  __syncthreads();
  constexpr int NK = D_ / 64;
  for (int kt = 0; kt < NK; ++kt) {
    if (kt + 1 < NK) gload((kt + 1) * 64);
    const u16* st = smem + (kt & 1) * C::STAGE_ELEMS;
    mma_step<128, 256, 2, 4>(st, st + C::A_ELEMS, acc, wm, wn, lane);
    if (kt + 1 < NK) swrite(smem + ((kt + 1) & 1) * C::STAGE_ELEMS, (kt + 1) * 64);
    __syncthreads();
  }
#pragma unroll
  for (int mt = 0; mt < C::MT; ++mt)
#pragma unroll
    for (int nt = 0; nt < 2; ++nt) {
      const int m = wm * 64 + mt * 16 + (lane & 15), n = wn * 32 + nt * 16 + (lane >> 4) * 4;
      float r[4];
#pragma unroll
      for (int j = 0; j < 4; ++j) {
        const float g = rbf(acc[mt][nt][j]), u = rbf(acc[mt][nt + 2][j]);
        const float y = 0.7978845608028654f * (g + 0.044715f * g * g * g);
        const float th = 1.f - 2.f / (__expf(2.f * y) + 1.f);
        const float ge = rbf(0.5f * g * (1.f + th));
        r[j] = ge * u;
      }
      u32x2 w; w[0] = pack2(r[0], r[1]); w[1] = pack2(r[2], r[3]);
      *(u32x2*)(hh + (size_t)(m0 + m) * DF_ + f0 + n) = w;
    }
}

__global__ __launch_bounds__(256) void down_kernel(const u16* __restrict__ hh, const u16* __restrict__ wd,
                                                   const u16* __restrict__ x1, const float* __restrict__ mod,
                                                   u16* __restrict__ out) {
  using C = Core<64, 64, 2, 2>;
  __shared__ __attribute__((aligned(16))) u16 smem[2 * C::STAGE_ELEMS];
  const int tid = threadIdx.x, lane = tid & 63, wid = tid >> 6, wm = wid >> 1, wn = wid & 1;
  const int rb = blockIdx.x >> 3, cb = blockIdx.x & 7;
  const int m0 = rb * 64, n0 = cb * 64;
  u32x4 ra[2], rbv[2];
  auto gload = [&](int k0) {
#pragma unroll
    for (int i = 0; i < 2; ++i) {
      const int ch = tid + i * 256;
      ra[i] = *(const u32x4*)(hh + (size_t)(m0 + (ch >> 3)) * DF_ + k0 + (ch & 7) * 8);
      rbv[i] = *(const u32x4*)(wd + (size_t)(k0 + (ch >> 3)) * D_ + n0 + (ch & 7) * 8);
    }
  };
  auto swrite = [&](u16* st) {
#pragma unroll
    for (int i = 0; i < 2; ++i) {
      const int ch = tid + i * 256;
      *(u32x4*)(st + (ch >> 3) * C::LDA + (ch & 7) * 8) = ra[i];
      *(u32x4*)(st + C::A_ELEMS + (ch >> 3) * C::LDB + (ch & 7) * 8) = rbv[i];
    }
  };
  f32x4 acc[2][2];
#pragma unroll
  for (int a = 0; a < 2; ++a)
#pragma unroll
    for (int b = 0; b < 2; ++b) acc[a][b] = (f32x4){0.f, 0.f, 0.f, 0.f};
  gload(0);
  swrite(smem);
  __syncthreads();
  constexpr int NK = DF_ / 64;
  for (int kt = 0; kt < NK; ++kt) {
    if (kt + 1 < NK) gload((kt + 1) * 64);
    const u16* st = smem + (kt & 1) * C::STAGE_ELEMS;
    mma_step<64, 64, 2, 2>(st, st + C::A_ELEMS, acc, wm, wn, lane);
    if (kt + 1 < NK) swrite(smem + ((kt + 1) & 1) * C::STAGE_ELEMS);
    __syncthreads();
  }
#pragma unroll
  for (int mt = 0; mt < 2; ++mt)
#pragma unroll
    for (int nt = 0; nt < 2; ++nt) {
      const int m = wm * 32 + mt * 16 + (lane & 15), n = wn * 32 + nt * 16 + (lane >> 4) * 4;
      const u32x2 xv = *(const u32x2*)(x1 + (size_t)(m0 + m) * D_ + n0 + n);
      const float4 gbv = *(const float4*)(mod + 5 * D_ + n0 + n);
      u32x2 w;
      w[0] = pack2(lo16(xv[0]) + rbf(gbv.x * rbf(acc[mt][nt][0])), hi16(xv[0]) + rbf(gbv.y * rbf(acc[mt][nt][1])));
      w[1] = pack2(lo16(xv[1]) + rbf(gbv.z * rbf(acc[mt][nt][2])), hi16(xv[1]) + rbf(gbv.w * rbf(acc[mt][nt][3])));
      *(u32x2*)(out + (size_t)(m0 + m) * D_ + n0 + n) = w;
    }
}

extern "C" void kernel_launch(void* const* d_in, const int* in_sizes, int n_in, void* d_out, int out_size,
                              void* d_ws, size_t ws_size, hipStream_t stream) {
  const u16* x = (const u16*)d_in[0];
  const u16* wqkv = (const u16*)d_in[1];
  const u16* wo = (const u16*)d_in[2];
  const u16* wg = (const u16*)d_in[4];
  const u16* wu = (const u16*)d_in[5];
  const u16* wd = (const u16*)d_in[6];
  const u16* c = (const u16*)d_in[7];
  const u16* wmod = (const u16*)d_in[8];
  u16* out = (u16*)d_out;
  char* ws = (char*)d_ws;
  float* mod = (float*)ws;
  float2* stats = (float2*)(ws + 16384);
  u16* xn = (u16*)(ws + 16384 + 131072);
  u16* o = xn + (size_t)S_ * D_;
  u16* x1 = o + (size_t)S_ * D_;
  u16* hh = x1 + (size_t)S_ * D_;

  prep_kernel<<<PREP_MOD_BLOCKS + 256, 256, 0, stream>>>(x, c, wmod, mod, xn);
  qkv_attn_kernel<<<256, 256, 0, stream>>>(xn, wqkv, mod, o);
  wo_kernel<<<256, 256, 0, stream>>>(o, wo, x, mod, x1, stats);
  gateup_kernel<<<16 * 14, 512, 0, stream>>>(x1, stats, wg, wu, mod, hh);
  down_kernel<<<256, 256, 0, stream>>>(hh, wd, x1, mod, out);
}
```

```cpp
#include <hip/hip_runtime.h>
#include <hip/hip_bf16.h>
#include <stdint.h>


typedef unsigned short u16;
typedef short bf16x8 __attribute__((ext_vector_type(8)));
typedef short bf16x4 __attribute__((ext_vector_type(4)));
typedef float f32x4 __attribute__((ext_vector_type(4)));
typedef unsigned u32x4 __attribute__((ext_vector_type(4)));
typedef unsigned u32x2 __attribute__((ext_vector_type(2)));
typedef __attribute__((address_space(3))) bf16x4 lds_bf16x4;

constexpr int S_ = 2048, D_ = 512, DF_ = 1792;

__device__ __forceinline__ float bf2f(u16 v) { return __uint_as_float(((unsigned)v) << 16); }
__device__ __forceinline__ u16 f2bf(float f) { __bf16 b = (__bf16)f; return __builtin_bit_cast(u16, b); }
__device__ __forceinline__ float rbf(float f) { return bf2f(f2bf(f)); }
__device__ __forceinline__ float lo16(unsigned u) { return __uint_as_float(u << 16); }
__device__ __forceinline__ float hi16(unsigned u) { return __uint_as_float(u & 0xffff0000u); }
__device__ __forceinline__ unsigned pack2(float a, float b) { return (unsigned)f2bf(a) | ((unsigned)f2bf(b) << 16); }

__device__ __forceinline__ bf16x4 tr_read(const u16* p) {
  return __builtin_amdgcn_ds_read_tr16_b64_v4i16((lds_bf16x4*)p);
}


typedef __attribute__((address_space(3))) void lds_void;
__device__ __forceinline__ void glds16(const u16* g, u16* l) {
  __builtin_amdgcn_global_load_lds((const void*)g, (lds_void*)l, 16, 0, 0);
}
template <int N> __device__ __forceinline__ void wait_vm() { asm volatile("s_waitcnt vmcnt(%0)" ::"n"(N) : "memory"); }
__device__ __forceinline__ void raw_barrier() {
  asm volatile("s_waitcnt lgkmcnt(0)" ::: "memory");
  __builtin_amdgcn_s_barrier();
  asm volatile("" ::: "memory");
}
__device__ __forceinline__ int swzA(int row) { return row & 7; }
__device__ __forceinline__ int swzB(int row) { return (((row >> 1) & 1) << 1) | (((row >> 3) & 1) << 2); }

template <int MT, int NTL>
__device__ __forceinline__ void mma64(const u16* __restrict__ As, const u16* __restrict__ Bs, f32x4 (&acc)[MT][NTL],
                                      int mt0, int nt0, int lane) {
  const int g = lane >> 4, q = (lane & 15) >> 2, p = lane & 3;
#pragma unroll
  for (int ks = 0; ks < 2; ++ks) {
    bf16x8 a[MT], w[NTL];
#pragma unroll
    for (int mt = 0; mt < MT; ++mt) {
      const int row = (mt0 + mt) * 16 + (lane & 15);
      a[mt] = *(const bf16x8*)(As + row * 64 + (((ks * 4 + g) ^ swzA(row)) * 8));
    }
#pragma unroll
    for (int nt = 0; nt < NTL; ++nt) {
      const int ntg = nt0 + nt;
      const u16* bimg = Bs + (ntg >> 2) * 4096;
      const int r0 = ks * 32 + 8 * g + q, r1 = r0 + 4;
      const int ch = (ntg & 3) * 2 + (p >> 1);
      const bf16x4 t0 = tr_read(bimg + r0 * 64 + ((ch ^ swzB(r0)) * 8) + (p & 1) * 4);
      const bf16x4 t1 = tr_read(bimg + r1 * 64 + ((ch ^ swzB(r1)) * 8) + (p & 1) * 4);
      w[nt] = __builtin_shufflevector(t0, t1, 0, 1, 2, 3, 4, 5, 6, 7);
    }
#pragma unroll
    for (int mt = 0; mt < MT; ++mt)
#pragma unroll
      for (int nt = 0; nt < NTL; ++nt)
        acc[mt][nt] = __builtin_amdgcn_mfma_f32_16x16x32_bf16(w[nt], a[mt], acc[mt][nt], 0, 0, 0);
  }
}

template <int K, int NSLOT>
__device__ __forceinline__ void gemm64_dma(const u16* __restrict__ A, int lda, const u16* __restrict__ B, int ldb,
                                           u16* smem, f32x4 (&acc)[2][2], int tid) {
  constexpr int NK = K / 64, DEPTH = NSLOT - 1;
  const int lane = tid & 63, wid = tid >> 6, wm = wid >> 1, wn = wid & 1;
  int aoff[2], boff[2];
#pragma unroll
  for (int i = 0; i < 2; ++i) {
    const int s = (i * 4 + wid) * 64 + lane, row = s >> 3, pos = s & 7;
    aoff[i] = row * lda + ((pos ^ swzA(row)) * 8);
    boff[i] = row * ldb + ((pos ^ swzB(row)) * 8);
  }
  auto issue = [&](int kt) {
    u16* st = smem + (kt % NSLOT) * 8192;
#pragma unroll
    for (int i = 0; i < 2; ++i) glds16(A + kt * 64 + aoff[i], st + (i * 4 + wid) * 512);
#pragma unroll
    for (int i = 0; i < 2; ++i) glds16(B + (size_t)kt * 64 * ldb + boff[i], st + 4096 + (i * 4 + wid) * 512);
  };
#pragma unroll
  for (int s = 0; s < DEPTH; ++s) issue(s);
  for (int kt = 0; kt < NK - DEPTH + 1; ++kt) {
    wait_vm<(DEPTH - 1) * 4>();
    raw_barrier();
    if (kt + DEPTH < NK) issue(kt + DEPTH);
    const u16* st = smem + (kt % NSLOT) * 8192;
    mma64<2, 2>(st, st + 4096, acc, wm * 2, wn * 2, lane);
  }
#pragma unroll
  for (int kt = NK - DEPTH + 1; kt < NK; ++kt) {
    wait_vm<0>();
    raw_barrier();
    const u16* st = smem + (kt % NSLOT) * 8192;
    mma64<2, 2>(st, st + 4096, acc, wm * 2, wn * 2, lane);
  }
}

constexpr int PREP_MOD_BLOCKS = 96;
__global__ __launch_bounds__(256) void prep_kernel(const u16* __restrict__ x, const u16* __restrict__ c,
                                                   const u16* __restrict__ wmod, float* __restrict__ mod,
                                                   u16* __restrict__ xn) {
  __shared__ float red[64][33];
  const int tid = threadIdx.x;
  if (blockIdx.x < PREP_MOD_BLOCKS) {
    const int n0 = blockIdx.x * 32;
    const int cg = tid & 3, kl = tid >> 2;
    float acc[8];
#pragma unroll
    for (int j = 0; j < 8; ++j) acc[j] = 0.f;
#pragma unroll
    for (int i = 0; i < 8; ++i) {
      const int k = kl + 64 * i;
      const float cv = bf2f(c[k]);
      const float sv = rbf(cv / (1.f + __expf(-cv)));
      const u32x4 w = *(const u32x4*)(wmod + (size_t)k * 3072 + n0 + cg * 8);
#pragma unroll
      for (int j = 0; j < 4; ++j) {
        acc[2 * j] += sv * lo16(w[j]);
        acc[2 * j + 1] += sv * hi16(w[j]);
      }
    }
#pragma unroll
    for (int j = 0; j < 8; ++j) red[kl][cg * 8 + j] = acc[j];
    __syncthreads();
    if (tid < 32) {
      float s = 0.f;
#pragma unroll 8
      for (int i = 0; i < 64; ++i) s += red[i][tid];
      mod[n0 + tid] = rbf(s);
    }
  } else {
    const int b = blockIdx.x - PREP_MOD_BLOCKS;
    const int wave = tid >> 6, lane = tid & 63;
#pragma unroll
    for (int rr = 0; rr < 2; ++rr) {
      const int row = b * 8 + wave * 2 + rr;
      const u32x4 v = *(const u32x4*)(x + (size_t)row * D_ + lane * 8);
      float f[8];
#pragma unroll
      for (int j = 0; j < 4; ++j) { f[2 * j] = lo16(v[j]); f[2 * j + 1] = hi16(v[j]); }
      float s = 0.f;
#pragma unroll
      for (int j = 0; j < 8; ++j) s += f[j];
#pragma unroll
      for (int o = 32; o >= 1; o >>= 1) s += __shfl_xor(s, o);
      const float mean = s * (1.f / D_);
      float q = 0.f;
#pragma unroll
      for (int j = 0; j < 8; ++j) { const float d = f[j] - mean; q += d * d; }
#pragma unroll
      for (int o = 32; o >= 1; o >>= 1) q += __shfl_xor(q, o);
      const float var = rbf(q * (1.f / D_));
      const float mb = rbf(mean);
      const float den = rbf(sqrtf(rbf(var + 1e-5f)));
      u32x4 o;
#pragma unroll
      for (int j = 0; j < 4; ++j) o[j] = pack2(rbf(f[2 * j] - mb) / den, rbf(f[2 * j + 1] - mb) / den);
      *(u32x4*)(xn + (size_t)row * D_ + lane * 8) = o;
    }
  }
}

template <int BM, int BN, int WM, int WN>
struct Core {
  static constexpr int BK = 64;
  static constexpr int NT = WM * WN * 64;
  static constexpr int LDA = BK + 8;
  static constexpr int LDB = BN + 16;
  static constexpr int A_ELEMS = BM * LDA, B_ELEMS = BK * LDB;
  static constexpr int STAGE_ELEMS = A_ELEMS + B_ELEMS;
  static constexpr int ACH = BM * (BK / 8), BCH = BK * (BN / 8);
  static constexpr int AIT = (ACH + NT - 1) / NT, BIT = (BCH + NT - 1) / NT;
  static constexpr int MT = BM / WM / 16, NTL = BN / WN / 16;
};

template <int BM, int BN, int WM, int WN>
__device__ __forceinline__ void mma_step(const u16* __restrict__ As, const u16* __restrict__ Bs,
                                         f32x4 (&acc)[Core<BM, BN, WM, WN>::MT][Core<BM, BN, WM, WN>::NTL],
                                         int wm, int wn, int lane) {
  using C = Core<BM, BN, WM, WN>;
  const int g = lane >> 4, q = (lane & 15) >> 2, p = lane & 3;
#pragma unroll
  for (int ks = 0; ks < 2; ++ks) {
    bf16x8 a[C::MT], w[C::NTL];
#pragma unroll
    for (int mt = 0; mt < C::MT; ++mt)
      a[mt] = *(const bf16x8*)(As + (wm * (BM / WM) + mt * 16 + (lane & 15)) * C::LDA + ks * 32 + g * 8);
#pragma unroll
    for (int nt = 0; nt < C::NTL; ++nt) {
      const u16* bp = Bs + (ks * 32 + 8 * g + q) * C::LDB + wn * (BN / WN) + nt * 16 + 4 * p;
      const bf16x4 t0 = tr_read(bp), t1 = tr_read(bp + 4 * C::LDB);
      w[nt] = __builtin_shufflevector(t0, t1, 0, 1, 2, 3, 4, 5, 6, 7);
    }
#pragma unroll
    for (int mt = 0; mt < C::MT; ++mt)
#pragma unroll
      for (int nt = 0; nt < C::NTL; ++nt)
        acc[mt][nt] = __builtin_amdgcn_mfma_f32_16x16x32_bf16(w[nt], a[mt], acc[mt][nt], 0, 0, 0);
  }
}

__global__ __launch_bounds__(256) void qkv_attn_kernel(const u16* __restrict__ xn, const u16* __restrict__ wqkv,
                                                       const float* __restrict__ mod, u16* __restrict__ o) {
  using C = Core<80, 192, 1, 4>;
  constexpr int TLD = 200;
  constexpr int SMEM_ELEMS = (2 * C::STAGE_ELEMS > 80 * TLD) ? 2 * C::STAGE_ELEMS : 80 * TLD;
  __shared__ __attribute__((aligned(16))) u16 smem[SMEM_ELEMS];
  __shared__ float s_opc[D_], s_sa[D_];
  const int tid = threadIdx.x, lane = tid & 63, wn = tid >> 6;
  const int rb = blockIdx.x >> 3, hg = blockIdx.x & 7;
  const int m0 = rb * 64;
  for (int i = tid; i < D_; i += 256) { s_sa[i] = mod[i]; s_opc[i] = rbf(1.f + mod[D_ + i]); }

  u32x4 ra[C::AIT], rbv[C::BIT];
  auto gload = [&](int k0) {
#pragma unroll
    for (int i = 0; i < C::AIT; ++i) {
      const int ch = tid + i * 256;
      if (ch < C::ACH) {
        const int r = ch >> 3, kc = ch & 7;
        int grow = m0 - 16 + r; grow = grow < 0 ? 0 : grow;
        ra[i] = *(const u32x4*)(xn + (size_t)grow * D_ + k0 + kc * 8);
      }
    }
#pragma unroll
    for (int i = 0; i < C::BIT; ++i) {
      const int ch = tid + i * 256;
      const int kr = ch / 24, nc = ch % 24;
      const int seg = nc >> 3, c8 = nc & 7;
      rbv[i] = *(const u32x4*)(wqkv + (size_t)(k0 + kr) * 1536 + seg * 512 + hg * 64 + c8 * 8);
    }
  };
  auto swrite = [&](u16* st, int k0) {
    u16* As = st; u16* Bs = st + C::A_ELEMS;
#pragma unroll
    for (int i = 0; i < C::AIT; ++i) {
      const int ch = tid + i * 256;
      if (ch < C::ACH) {
        const int r = ch >> 3, kc = ch & 7;
        u32x4 w;
#pragma unroll
        for (int j = 0; j < 4; ++j) {
          const int k = k0 + kc * 8 + 2 * j;
          const float a0 = rbf(rbf(lo16(ra[i][j]) * s_opc[k]) + s_sa[k]);
          const float a1 = rbf(rbf(hi16(ra[i][j]) * s_opc[k + 1]) + s_sa[k + 1]);
          w[j] = pack2(a0, a1);
        }
        *(u32x4*)(As + r * C::LDA + kc * 8) = w;
      }
    }
#pragma unroll
    for (int i = 0; i < C::BIT; ++i) {
      const int ch = tid + i * 256;
      const int kr = ch / 24, nc = ch % 24;
      *(u32x4*)(Bs + kr * C::LDB + nc * 8) = rbv[i];
    }
  };

  f32x4 acc[C::MT][C::NTL];
#pragma unroll
  for (int a = 0; a < C::MT; ++a)
#pragma unroll
    for (int b = 0; b < C::NTL; ++b) acc[a][b] = (f32x4){0.f, 0.f, 0.f, 0.f};

  gload(0);
  __syncthreads();
  swrite(smem, 0);
  __syncthreads();
  constexpr int NK = D_ / 64;
  for (int kt = 0; kt < NK; ++kt) {
    if (kt + 1 < NK) gload((kt + 1) * 64);
    const u16* st = smem + (kt & 1) * C::STAGE_ELEMS;
    mma_step<80, 192, 1, 4>(st, st + C::A_ELEMS, acc, 0, wn, lane);
    if (kt + 1 < NK) swrite(smem + ((kt + 1) & 1) * C::STAGE_ELEMS, (kt + 1) * 64);
    __syncthreads();
  }
#pragma unroll
  for (int mt = 0; mt < C::MT; ++mt)
#pragma unroll
    for (int nt = 0; nt < C::NTL; ++nt) {
      const int m = mt * 16 + (lane & 15), n = wn * 48 + nt * 16 + (lane >> 4) * 4;
      u32x2 w;
      w[0] = pack2(acc[mt][nt][0], acc[mt][nt][1]);
      w[1] = pack2(acc[mt][nt][2], acc[mt][nt][3]);
      *(u32x2*)(smem + m * TLD + n) = w;
    }
  __syncthreads();
  {
    const int r = tid >> 2, hl = tid & 3;
    const u16* qp = smem + (16 + r) * TLD + hl * 16;
    float q[16];
    {
      const u32x4 v0 = *(const u32x4*)qp, v1 = *(const u32x4*)(qp + 8);
#pragma unroll
      for (int j = 0; j < 4; ++j) { q[2 * j] = lo16(v0[j]); q[2 * j + 1] = hi16(v0[j]); q[8 + 2 * j] = lo16(v1[j]); q[9 + 2 * j] = hi16(v1[j]); }
    }
    float lg[8];
    float mx = -3.0e38f;
#pragma unroll
    for (int w = 0; w < 8; ++w) {
      const int row = 16 + r - 2 * w;
      const u16* kp = smem + row * TLD + 64 + hl * 16;
      const u32x4 v0 = *(const u32x4*)kp, v1 = *(const u32x4*)(kp + 8);
      float d = 0.f;
#pragma unroll
      for (int j = 0; j < 4; ++j) {
        d += q[2 * j] * lo16(v0[j]); d += q[2 * j + 1] * hi16(v0[j]);
        d += q[8 + 2 * j] * lo16(v1[j]); d += q[9 + 2 * j] * hi16(v1[j]);
      }
      float l = rbf(rbf(d) * 0.25f);
      if (m0 + r - 2 * w < 0) l = rbf(-1000000000.0f);
      lg[w] = l;
      mx = fmaxf(mx, l);
    }
    float sum = 0.f;
#pragma unroll
    for (int w = 0; w < 8; ++w) { lg[w] = __expf(lg[w] - mx); sum += lg[w]; }
    const float inv = 1.f / sum;
    float ov[16];
#pragma unroll
    for (int j = 0; j < 16; ++j) ov[j] = 0.f;
#pragma unroll
    for (int w = 0; w < 8; ++w) {
      const int row = 16 + r - 2 * w;
      const float pw = rbf(lg[w] * inv);
      const u16* vp = smem + row * TLD + 128 + hl * 16;
      const u32x4 v0 = *(const u32x4*)vp, v1 = *(const u32x4*)(vp + 8);
#pragma unroll
      for (int j = 0; j < 4; ++j) {
        ov[2 * j] += pw * lo16(v0[j]); ov[2 * j + 1] += pw * hi16(v0[j]);
        ov[8 + 2 * j] += pw * lo16(v1[j]); ov[9 + 2 * j] += pw * hi16(v1[j]);
      }
    }
    u32x4 o0, o1;
#pragma unroll
    for (int j = 0; j < 4; ++j) { o0[j] = pack2(ov[2 * j], ov[2 * j + 1]); o1[j] = pack2(ov[8 + 2 * j], ov[9 + 2 * j]); }
    u16* op = o + (size_t)(m0 + r) * D_ + hg * 64 + hl * 16;
    *(u32x4*)op = o0;
    *(u32x4*)(op + 8) = o1;
  }
}

__global__ __launch_bounds__(256) void wo_kernel(const u16* __restrict__ o, const u16* __restrict__ wo,
                                                 const u16* __restrict__ x, const float* __restrict__ mod,
                                                 u16* __restrict__ x1, float2* __restrict__ stats) {
  constexpr int NSLOT = 8;
  __shared__ __attribute__((aligned(16))) u16 smem[NSLOT * 8192];
  const int tid = threadIdx.x, lane = tid & 63, wid = tid >> 6, wm = wid >> 1, wn = wid & 1;
  const int rb = blockIdx.x >> 3, cb = blockIdx.x & 7;
  const int m0 = rb * 64, n0 = cb * 64;
  f32x4 acc[2][2];
#pragma unroll
  for (int a = 0; a < 2; ++a)
#pragma unroll
    for (int b = 0; b < 2; ++b) acc[a][b] = (f32x4){0.f, 0.f, 0.f, 0.f};
  gemm64_dma<D_, NSLOT>(o + (size_t)m0 * D_, D_, wo + n0, D_, smem, acc, tid);
  __syncthreads();
  float* T = (float*)smem;
#pragma unroll
  for (int mt = 0; mt < 2; ++mt)
#pragma unroll
    for (int nt = 0; nt < 2; ++nt) {
      const int m = wm * 32 + mt * 16 + (lane & 15), n = wn * 32 + nt * 16 + (lane >> 4) * 4;
      const u32x2 xv = *(const u32x2*)(x + (size_t)(m0 + m) * D_ + n0 + n);
      float r[4];
      const float4 gav = *(const float4*)(mod + 2 * D_ + n0 + n);
      r[0] = rbf(lo16(xv[0]) + rbf(gav.x * rbf(acc[mt][nt][0])));
      r[1] = rbf(hi16(xv[0]) + rbf(gav.y * rbf(acc[mt][nt][1])));
      r[2] = rbf(lo16(xv[1]) + rbf(gav.z * rbf(acc[mt][nt][2])));
      r[3] = rbf(hi16(xv[1]) + rbf(gav.w * rbf(acc[mt][nt][3])));
      u32x2 w; w[0] = pack2(r[0], r[1]); w[1] = pack2(r[2], r[3]);
      *(u32x2*)(x1 + (size_t)(m0 + m) * D_ + n0 + n) = w;
#pragma unroll
      for (int j = 0; j < 4; ++j) T[m * 65 + n + j] = r[j];
    }
  __syncthreads();
  if (tid < 64) {
    float s = 0.f;
#pragma unroll 16
    for (int j = 0; j < 64; ++j) s += T[tid * 65 + j];
    const float mean = s * (1.f / 64.f);
    float q = 0.f;
#pragma unroll 16
    for (int j = 0; j < 64; ++j) { const float d = T[tid * 65 + j] - mean; q += d * d; }
    stats[(size_t)(m0 + tid) * 8 + cb] = make_float2(mean, q);
  }
}

__global__ __launch_bounds__(512) void gateup_kernel(const u16* __restrict__ x1, const float2* __restrict__ stats,
                                                     const u16* __restrict__ wg, const u16* __restrict__ wu,
                                                     const float* __restrict__ mod, u16* __restrict__ hh) {
  using C = Core<128, 256, 2, 4>;
  __shared__ __attribute__((aligned(16))) u16 smem[2 * C::STAGE_ELEMS];
  __shared__ float s_opc[D_], s_sb[D_], s_mean[128], s_den[128];
  const int tid = threadIdx.x, lane = tid & 63, wid = tid >> 6, wm = wid >> 2, wn = wid & 3;
  const int rb = blockIdx.x / 14, cbk = blockIdx.x % 14;
  const int m0 = rb * 128, f0 = cbk * 128;
  for (int i = tid; i < D_; i += 512) { s_sb[i] = mod[3 * D_ + i]; s_opc[i] = rbf(1.f + mod[4 * D_ + i]); }
  if (tid < 128) {
    const float2* sp = stats + (size_t)(m0 + tid) * 8;
    float mi[8], s = 0.f, q = 0.f;
#pragma unroll
    for (int j = 0; j < 8; ++j) { const float2 v = sp[j]; mi[j] = v.x; s += v.x; q += v.y; }
    const float mean = s * 0.125f;
#pragma unroll
    for (int j = 0; j < 8; ++j) { const float d = mi[j] - mean; q += 64.f * d * d; }
    const float var = rbf(q * (1.f / D_));
    s_mean[tid] = rbf(mean);
    s_den[tid] = rbf(sqrtf(rbf(var + 1e-5f)));
  }
  u32x4 ra[2], rbv[4];
  auto gload = [&](int k0) {
#pragma unroll
    for (int i = 0; i < 2; ++i) {
      const int ch = tid + i * 512;
      ra[i] = *(const u32x4*)(x1 + (size_t)(m0 + (ch >> 3)) * D_ + k0 + (ch & 7) * 8);
    }
#pragma unroll
    for (int i = 0; i < 4; ++i) {
      const int ch = tid + i * 512;
      const int kr = ch >> 5, nc = ch & 31;
      const int w = nc >> 3, isup = (nc >> 2) & 1, c8 = nc & 3;
      const u16* src = isup ? wu : wg;
      rbv[i] = *(const u32x4*)(src + (size_t)(k0 + kr) * DF_ + f0 + w * 32 + c8 * 8);
    }
  };
  auto swrite = [&](u16* st, int k0) {
#pragma unroll
    for (int i = 0; i < 2; ++i) {
      const int ch = tid + i * 512;
      const int r = ch >> 3, kc = ch & 7;
      const float mb = s_mean[r], den = s_den[r];
      u32x4 w;
#pragma unroll
      for (int j = 0; j < 4; ++j) {
        const int k = k0 + kc * 8 + 2 * j;
        const float a0 = rbf(rbf(rbf(rbf(lo16(ra[i][j]) - mb) / den) * s_opc[k]) + s_sb[k]);
        const float a1 = rbf(rbf(rbf(rbf(hi16(ra[i][j]) - mb) / den) * s_opc[k + 1]) + s_sb[k + 1]);
        w[j] = pack2(a0, a1);
      }
      *(u32x4*)(st + r * C::LDA + kc * 8) = w;
    }
#pragma unroll
    for (int i = 0; i < 4; ++i) {
      const int ch = tid + i * 512;
      *(u32x4*)(st + C::A_ELEMS + (ch >> 5) * C::LDB + (ch & 31) * 8) = rbv[i];
    }
  };
  f32x4 acc[C::MT][C::NTL];
#pragma unroll
  for (int a = 0; a < C::MT; ++a)
#pragma unroll
    for (int b = 0; b < C::NTL; ++b) acc[a][b] = (f32x4){0.f, 0.f, 0.f, 0.f};
  gload(0);
  __syncthreads();
  swrite(smem, 0);
  __syncthreads();
  constexpr int NK = D_ / 64;
  for (int kt = 0; kt < NK; ++kt) {
    if (kt + 1 < NK) gload((kt + 1) * 64);
    const u16* st = smem + (kt & 1) * C::STAGE_ELEMS;
    mma_step<128, 256, 2, 4>(st, st + C::A_ELEMS, acc, wm, wn, lane);
    if (kt + 1 < NK) swrite(smem + ((kt + 1) & 1) * C::STAGE_ELEMS, (kt + 1) * 64);
    __syncthreads();
  }
#pragma unroll
  for (int mt = 0; mt < C::MT; ++mt)
#pragma unroll
    for (int nt = 0; nt < 2; ++nt) {
      const int m = wm * 64 + mt * 16 + (lane & 15), n = wn * 32 + nt * 16 + (lane >> 4) * 4;
      float r[4];
#pragma unroll
      for (int j = 0; j < 4; ++j) {
        const float g = rbf(acc[mt][nt][j]), u = rbf(acc[mt][nt + 2][j]);
        const float y = 0.7978845608028654f * (g + 0.044715f * g * g * g);
        const float th = 1.f - 2.f / (__expf(2.f * y) + 1.f);
        const float ge = rbf(0.5f * g * (1.f + th));
        r[j] = ge * u;
      }
      u32x2 w; w[0] = pack2(r[0], r[1]); w[1] = pack2(r[2], r[3]);
      *(u32x2*)(hh + (size_t)(m0 + m) * DF_ + f0 + n) = w;
    }
}

__global__ __launch_bounds__(256) void down_kernel(const u16* __restrict__ hh, const u16* __restrict__ wd,
                                                   const u16* __restrict__ x1, const float* __restrict__ mod,
                                                   u16* __restrict__ out) {
  constexpr int NSLOT = 8;
  __shared__ __attribute__((aligned(16))) u16 smem[NSLOT * 8192];
  const int tid = threadIdx.x, lane = tid & 63, wid = tid >> 6, wm = wid >> 1, wn = wid & 1;
  const int rb = blockIdx.x >> 3, cb = blockIdx.x & 7;
  const int m0 = rb * 64, n0 = cb * 64;
  f32x4 acc[2][2];
#pragma unroll
  for (int a = 0; a < 2; ++a)
#pragma unroll
    for (int b = 0; b < 2; ++b) acc[a][b] = (f32x4){0.f, 0.f, 0.f, 0.f};
  gemm64_dma<DF_, NSLOT>(hh + (size_t)m0 * DF_, DF_, wd + n0, D_, smem, acc, tid);
#pragma unroll
  for (int mt = 0; mt < 2; ++mt)
#pragma unroll
    for (int nt = 0; nt < 2; ++nt) {
      const int m = wm * 32 + mt * 16 + (lane & 15), n = wn * 32 + nt * 16 + (lane >> 4) * 4;
      const u32x2 xv = *(const u32x2*)(x1 + (size_t)(m0 + m) * D_ + n0 + n);
      const float4 gbv = *(const float4*)(mod + 5 * D_ + n0 + n);
      u32x2 w;
      w[0] = pack2(lo16(xv[0]) + rbf(gbv.x * rbf(acc[mt][nt][0])), hi16(xv[0]) + rbf(gbv.y * rbf(acc[mt][nt][1])));
      w[1] = pack2(lo16(xv[1]) + rbf(gbv.z * rbf(acc[mt][nt][2])), hi16(xv[1]) + rbf(gbv.w * rbf(acc[mt][nt][3])));
      *(u32x2*)(out + (size_t)(m0 + m) * D_ + n0 + n) = w;
    }
}

extern "C" void kernel_launch(void* const* d_in, const int* in_sizes, int n_in, void* d_out, int out_size,
                              void* d_ws, size_t ws_size, hipStream_t stream) {
  const u16* x = (const u16*)d_in[0];
  const u16* wqkv = (const u16*)d_in[1];
  const u16* wo = (const u16*)d_in[2];
  const u16* wg = (const u16*)d_in[4];
  const u16* wu = (const u16*)d_in[5];
  const u16* wd = (const u16*)d_in[6];
  const u16* c = (const u16*)d_in[7];
  const u16* wmod = (const u16*)d_in[8];
  u16* out = (u16*)d_out;
  char* ws = (char*)d_ws;
  float* mod = (float*)ws;
  float2* stats = (float2*)(ws + 16384);
  u16* xn = (u16*)(ws + 16384 + 131072);
  u16* o = xn + (size_t)S_ * D_;
  u16* x1 = o + (size_t)S_ * D_;
  u16* hh = x1 + (size_t)S_ * D_;

  prep_kernel<<<PREP_MOD_BLOCKS + 256, 256, 0, stream>>>(x, c, wmod, mod, xn);
  qkv_attn_kernel<<<256, 256, 0, stream>>>(xn, wqkv, mod, o);
  wo_kernel<<<256, 256, 0, stream>>>(o, wo, x, mod, x1, stats);
  gateup_kernel<<<16 * 14, 512, 0, stream>>>(x1, stats, wg, wu, mod, hh);
  down_kernel<<<256, 256, 0, stream>>>(hh, wd, x1, mod, out);
}
```

```cpp
#include <hip/hip_runtime.h>
#include <hip/hip_bf16.h>
#include <stdint.h>


typedef unsigned short u16;
typedef short bf16x8 __attribute__((ext_vector_type(8)));
typedef short bf16x4 __attribute__((ext_vector_type(4)));
typedef float f32x4 __attribute__((ext_vector_type(4)));
typedef unsigned u32x4 __attribute__((ext_vector_type(4)));
typedef unsigned u32x2 __attribute__((ext_vector_type(2)));
typedef __attribute__((address_space(3))) bf16x4 lds_bf16x4;

constexpr int S_ = 2048, D_ = 512, DF_ = 1792;

__device__ __forceinline__ float bf2f(u16 v) { return __uint_as_float(((unsigned)v) << 16); }
__device__ __forceinline__ u16 f2bf(float f) { __bf16 b = (__bf16)f; return __builtin_bit_cast(u16, b); }
__device__ __forceinline__ float rbf(float f) { return bf2f(f2bf(f)); }
__device__ __forceinline__ float lo16(unsigned u) { return __uint_as_float(u << 16); }
__device__ __forceinline__ float hi16(unsigned u) { return __uint_as_float(u & 0xffff0000u); }
__device__ __forceinline__ unsigned pack2(float a, float b) { return (unsigned)f2bf(a) | ((unsigned)f2bf(b) << 16); }

__device__ __forceinline__ bf16x4 tr_read(const u16* p) {
  return __builtin_amdgcn_ds_read_tr16_b64_v4i16((lds_bf16x4*)p);
}


typedef __attribute__((address_space(3))) void lds_void;
__device__ __forceinline__ void glds16(const u16* g, u16* l) {
  __builtin_amdgcn_global_load_lds((const void*)g, (lds_void*)l, 16, 0, 0);
}
template <int N> __device__ __forceinline__ void wait_vm() { asm volatile("s_waitcnt vmcnt(%0)" ::"n"(N) : "memory"); }
__device__ __forceinline__ void raw_barrier() {
  asm volatile("s_waitcnt lgkmcnt(0)" ::: "memory");
  __builtin_amdgcn_s_barrier();
  asm volatile("" ::: "memory");
}
__device__ __forceinline__ int swzA(int row) { return row & 7; }
__device__ __forceinline__ int swzB(int row) { return (((row >> 1) & 1) << 1) | (((row >> 3) & 1) << 2); }

template <int MT, int NTL>
__device__ __forceinline__ void mma64(const u16* __restrict__ As, const u16* __restrict__ Bs, f32x4 (&acc)[MT][NTL],
                                      int mt0, int nt0, int lane) {
  const int g = lane >> 4, q = (lane & 15) >> 2, p = lane & 3;
#pragma unroll
  for (int ks = 0; ks < 2; ++ks) {
    bf16x8 a[MT], w[NTL];
#pragma unroll
    for (int mt = 0; mt < MT; ++mt) {
      const int row = (mt0 + mt) * 16 + (lane & 15);
      a[mt] = *(const bf16x8*)(As + row * 64 + (((ks * 4 + g) ^ swzA(row)) * 8));
    }
#pragma unroll
    for (int nt = 0; nt < NTL; ++nt) {
      const int ntg = nt0 + nt;
      const u16* bimg = Bs + (ntg >> 2) * 4096;
      const int r0 = ks * 32 + 8 * g + q, r1 = r0 + 4;
      const int ch = (ntg & 3) * 2 + (p >> 1);
      const bf16x4 t0 = tr_read(bimg + r0 * 64 + ((ch ^ swzB(r0)) * 8) + (p & 1) * 4);
      const bf16x4 t1 = tr_read(bimg + r1 * 64 + ((ch ^ swzB(r1)) * 8) + (p & 1) * 4);
      w[nt] = __builtin_shufflevector(t0, t1, 0, 1, 2, 3, 4, 5, 6, 7);
    }
#pragma unroll
    for (int mt = 0; mt < MT; ++mt)
#pragma unroll
      for (int nt = 0; nt < NTL; ++nt)
        acc[mt][nt] = __builtin_amdgcn_mfma_f32_16x16x32_bf16(w[nt], a[mt], acc[mt][nt], 0, 0, 0);
  }
}

__device__ __forceinline__ void mma64_22(const u16* __restrict__ As, const u16* __restrict__ Bs, f32x4 (&acc)[2][2],
                                         int mt0, int nt0, int lane) {
  const int g = lane >> 4, q = (lane & 15) >> 2, p = lane & 3;
  bf16x8 a[2][2], w[2][2];
#pragma unroll
  for (int ks = 0; ks < 2; ++ks) {
#pragma unroll
    for (int mt = 0; mt < 2; ++mt) {
      const int row = (mt0 + mt) * 16 + (lane & 15);
      a[ks][mt] = *(const bf16x8*)(As + row * 64 + (((ks * 4 + g) ^ swzA(row)) * 8));
    }
#pragma unroll
    for (int nt = 0; nt < 2; ++nt) {
      const int r0 = ks * 32 + 8 * g + q, r1 = r0 + 4;
      const int ch = (nt0 + nt) * 2 + (p >> 1);
      const bf16x4 t0 = tr_read(Bs + r0 * 64 + ((ch ^ swzB(r0)) * 8) + (p & 1) * 4);
      const bf16x4 t1 = tr_read(Bs + r1 * 64 + ((ch ^ swzB(r1)) * 8) + (p & 1) * 4);
      w[ks][nt] = __builtin_shufflevector(t0, t1, 0, 1, 2, 3, 4, 5, 6, 7);
    }
  }
#pragma unroll
  for (int ks = 0; ks < 2; ++ks)
#pragma unroll
    for (int mt = 0; mt < 2; ++mt)
#pragma unroll
      for (int nt = 0; nt < 2; ++nt)
        acc[mt][nt] = __builtin_amdgcn_mfma_f32_16x16x32_bf16(w[ks][nt], a[ks][mt], acc[mt][nt], 0, 0, 0);
}

template <int K, int NSLOT>
__device__ __forceinline__ void gemm64_dma(const u16* __restrict__ A, int lda, const u16* __restrict__ B, int ldb,
                                           u16* smem, f32x4 (&acc)[2][2], int tid) {
  constexpr int NK = K / 64, DEPTH = NSLOT - 1;
  const int lane = tid & 63, wid = __builtin_amdgcn_readfirstlane(tid >> 6);
  if (wid >= 4) {
    const int lw = wid - 4;
    int aoff[2], boff[2];
#pragma unroll
    for (int i = 0; i < 2; ++i) {
      const int s = (i * 4 + lw) * 64 + lane, row = s >> 3, pos = s & 7;
      aoff[i] = row * lda + ((pos ^ swzA(row)) * 8);
      boff[i] = row * ldb + ((pos ^ swzB(row)) * 8);
    }
    auto issue = [&](int kt) {
      u16* st = smem + (kt % NSLOT) * 8192;
#pragma unroll
      for (int i = 0; i < 2; ++i) glds16(A + kt * 64 + aoff[i], st + (i * 4 + lw) * 512);
#pragma unroll
      for (int i = 0; i < 2; ++i) glds16(B + (size_t)kt * 64 * ldb + boff[i], st + 4096 + (i * 4 + lw) * 512);
    };
#pragma unroll
    for (int s = 0; s < DEPTH; ++s) issue(s);
    for (int kt = 0; kt < NK - DEPTH + 1; ++kt) {
      wait_vm<(DEPTH - 1) * 4>();
      raw_barrier();
      if (kt + DEPTH < NK) issue(kt + DEPTH);
    }
#pragma unroll
    for (int kt = NK - DEPTH + 1; kt < NK; ++kt) {
      wait_vm<0>();
      raw_barrier();
    }
  } else {
    const int wm = wid >> 1, wn = wid & 1;
    for (int kt = 0; kt < NK; ++kt) {
      raw_barrier();
      const u16* st = smem + (kt % NSLOT) * 8192;
      mma64_22(st, st + 4096, acc, wm * 2, wn * 2, lane);
    }
  }
}

constexpr int PREP_MOD_BLOCKS = 96;
__global__ __launch_bounds__(256) void prep_kernel(const u16* __restrict__ x, const u16* __restrict__ c,
                                                   const u16* __restrict__ wmod, float* __restrict__ mod,
                                                   u16* __restrict__ xn) {
  __shared__ float red[64][33];
  const int tid = threadIdx.x;
  if (blockIdx.x < PREP_MOD_BLOCKS) {
    const int n0 = blockIdx.x * 32;
    const int cg = tid & 3, kl = tid >> 2;
    float acc[8];
#pragma unroll
    for (int j = 0; j < 8; ++j) acc[j] = 0.f;
#pragma unroll
    for (int i = 0; i < 8; ++i) {
      const int k = kl + 64 * i;
      const float cv = bf2f(c[k]);
      const float sv = rbf(cv / (1.f + __expf(-cv)));
      const u32x4 w = *(const u32x4*)(wmod + (size_t)k * 3072 + n0 + cg * 8);
#pragma unroll
      for (int j = 0; j < 4; ++j) {
        acc[2 * j] += sv * lo16(w[j]);
        acc[2 * j + 1] += sv * hi16(w[j]);
      }
    }
#pragma unroll
    for (int j = 0; j < 8; ++j) red[kl][cg * 8 + j] = acc[j];
    __syncthreads();
    if (tid < 32) {
      float s = 0.f;
#pragma unroll 8
      for (int i = 0; i < 64; ++i) s += red[i][tid];
      mod[n0 + tid] = rbf(s);
    }
  } else {
    const int b = blockIdx.x - PREP_MOD_BLOCKS;
    const int wave = tid >> 6, lane = tid & 63;
#pragma unroll
    for (int rr = 0; rr < 2; ++rr) {
      const int row = b * 8 + wave * 2 + rr;
      const u32x4 v = *(const u32x4*)(x + (size_t)row * D_ + lane * 8);
      float f[8];
#pragma unroll
      for (int j = 0; j < 4; ++j) { f[2 * j] = lo16(v[j]); f[2 * j + 1] = hi16(v[j]); }
      float s = 0.f;
#pragma unroll
      for (int j = 0; j < 8; ++j) s += f[j];
#pragma unroll
      for (int o = 32; o >= 1; o >>= 1) s += __shfl_xor(s, o);
      const float mean = s * (1.f / D_);
      float q = 0.f;
#pragma unroll
      for (int j = 0; j < 8; ++j) { const float d = f[j] - mean; q += d * d; }
#pragma unroll
      for (int o = 32; o >= 1; o >>= 1) q += __shfl_xor(q, o);
      const float var = rbf(q * (1.f / D_));
      const float mb = rbf(mean);
      const float den = rbf(sqrtf(rbf(var + 1e-5f)));
      u32x4 o;
#pragma unroll
      for (int j = 0; j < 4; ++j) o[j] = pack2(rbf(f[2 * j] - mb) / den, rbf(f[2 * j + 1] - mb) / den);
      *(u32x4*)(xn + (size_t)row * D_ + lane * 8) = o;
    }
  }
}

template <int BM, int BN, int WM, int WN>
struct Core {
  static constexpr int BK = 64;
  static constexpr int NT = WM * WN * 64;
  static constexpr int LDA = BK + 8;
  static constexpr int LDB = BN + 16;
  static constexpr int A_ELEMS = BM * LDA, B_ELEMS = BK * LDB;
  static constexpr int STAGE_ELEMS = A_ELEMS + B_ELEMS;
  static constexpr int ACH = BM * (BK / 8), BCH = BK * (BN / 8);
  static constexpr int AIT = (ACH + NT - 1) / NT, BIT = (BCH + NT - 1) / NT;
  static constexpr int MT = BM / WM / 16, NTL = BN / WN / 16;
};

template <int BM, int BN, int WM, int WN>
__device__ __forceinline__ void mma_step(const u16* __restrict__ As, const u16* __restrict__ Bs,
                                         f32x4 (&acc)[Core<BM, BN, WM, WN>::MT][Core<BM, BN, WM, WN>::NTL],
                                         int wm, int wn, int lane) {
  using C = Core<BM, BN, WM, WN>;
  const int g = lane >> 4, q = (lane & 15) >> 2, p = lane & 3;
#pragma unroll
  for (int ks = 0; ks < 2; ++ks) {
    bf16x8 a[C::MT], w[C::NTL];
#pragma unroll
    for (int mt = 0; mt < C::MT; ++mt)
      a[mt] = *(const bf16x8*)(As + (wm * (BM / WM) + mt * 16 + (lane & 15)) * C::LDA + ks * 32 + g * 8);
#pragma unroll
    for (int nt = 0; nt < C::NTL; ++nt) {
      const u16* bp = Bs + (ks * 32 + 8 * g + q) * C::LDB + wn * (BN / WN) + nt * 16 + 4 * p;
      const bf16x4 t0 = tr_read(bp), t1 = tr_read(bp + 4 * C::LDB);
      w[nt] = __builtin_shufflevector(t0, t1, 0, 1, 2, 3, 4, 5, 6, 7);
    }
#pragma unroll
    for (int mt = 0; mt < C::MT; ++mt)
#pragma unroll
      for (int nt = 0; nt < C::NTL; ++nt)
        acc[mt][nt] = __builtin_amdgcn_mfma_f32_16x16x32_bf16(w[nt], a[mt], acc[mt][nt], 0, 0, 0);
  }
}

__global__ __launch_bounds__(256) void qkv_attn_kernel(const u16* __restrict__ xn, const u16* __restrict__ wqkv,
                                                       const float* __restrict__ mod, u16* __restrict__ o) {
  using C = Core<80, 192, 1, 4>;
  constexpr int TLD = 200;
  constexpr int SMEM_ELEMS = (2 * C::STAGE_ELEMS > 80 * TLD) ? 2 * C::STAGE_ELEMS : 80 * TLD;
  __shared__ __attribute__((aligned(16))) u16 smem[SMEM_ELEMS];
  __shared__ float s_opc[D_], s_sa[D_];
  const int tid = threadIdx.x, lane = tid & 63, wn = tid >> 6;
  const int xcd = blockIdx.x & 7, idx = blockIdx.x >> 3;
  const int rb = (xcd >> 1) * 8 + (idx >> 2), hg = (xcd & 1) * 4 + (idx & 3);
  const int m0 = rb * 64;
  for (int i = tid; i < D_; i += 256) { s_sa[i] = mod[i]; s_opc[i] = rbf(1.f + mod[D_ + i]); }

  u32x4 ra[C::AIT], rbv[C::BIT];
  auto gload = [&](int k0) {
#pragma unroll
    for (int i = 0; i < C::AIT; ++i) {
      const int ch = tid + i * 256;
      if (ch < C::ACH) {
        const int r = ch >> 3, kc = ch & 7;
        int grow = m0 - 16 + r; grow = grow < 0 ? 0 : grow;
        ra[i] = *(const u32x4*)(xn + (size_t)grow * D_ + k0 + kc * 8);
      }
    }
#pragma unroll
    for (int i = 0; i < C::BIT; ++i) {
      const int ch = tid + i * 256;
      const int kr = ch / 24, nc = ch % 24;
      const int seg = nc >> 3, c8 = nc & 7;
      rbv[i] = *(const u32x4*)(wqkv + (size_t)(k0 + kr) * 1536 + seg * 512 + hg * 64 + c8 * 8);
    }
  };
  auto swrite = [&](u16* st, int k0) {
    u16* As = st; u16* Bs = st + C::A_ELEMS;
#pragma unroll
    for (int i = 0; i < C::AIT; ++i) {
      const int ch = tid + i * 256;
      if (ch < C::ACH) {
        const int r = ch >> 3, kc = ch & 7;
        u32x4 w;
#pragma unroll
        for (int j = 0; j < 4; ++j) {
          const int k = k0 + kc * 8 + 2 * j;
          const float a0 = rbf(rbf(lo16(ra[i][j]) * s_opc[k]) + s_sa[k]);
          const float a1 = rbf(rbf(hi16(ra[i][j]) * s_opc[k + 1]) + s_sa[k + 1]);
          w[j] = pack2(a0, a1);
        }
        *(u32x4*)(As + r * C::LDA + kc * 8) = w;
      }
    }
#pragma unroll
    for (int i = 0; i < C::BIT; ++i) {
      const int ch = tid + i * 256;
      const int kr = ch / 24, nc = ch % 24;
      *(u32x4*)(Bs + kr * C::LDB + nc * 8) = rbv[i];
    }
  };

  f32x4 acc[C::MT][C::NTL];
#pragma unroll
  for (int a = 0; a < C::MT; ++a)
#pragma unroll
    for (int b = 0; b < C::NTL; ++b) acc[a][b] = (f32x4){0.f, 0.f, 0.f, 0.f};

  gload(0);
  __syncthreads();
  swrite(smem, 0);
  __syncthreads();
  constexpr int NK = D_ / 64;
  for (int kt = 0; kt < NK; ++kt) {
    if (kt + 1 < NK) gload((kt + 1) * 64);
    const u16* st = smem + (kt & 1) * C::STAGE_ELEMS;
    mma_step<80, 192, 1, 4>(st, st + C::A_ELEMS, acc, 0, wn, lane);
    if (kt + 1 < NK) swrite(smem + ((kt + 1) & 1) * C::STAGE_ELEMS, (kt + 1) * 64);
    __syncthreads();
  }
#pragma unroll
  for (int mt = 0; mt < C::MT; ++mt)
#pragma unroll
    for (int nt = 0; nt < C::NTL; ++nt) {
      const int m = mt * 16 + (lane & 15), n = wn * 48 + nt * 16 + (lane >> 4) * 4;
      u32x2 w;
      w[0] = pack2(acc[mt][nt][0], acc[mt][nt][1]);
      w[1] = pack2(acc[mt][nt][2], acc[mt][nt][3]);
      *(u32x2*)(smem + m * TLD + n) = w;
    }
  __syncthreads();
  {
    const int r = tid >> 2, hl = tid & 3;
    const u16* qp = smem + (16 + r) * TLD + hl * 16;
    float q[16];
    {
      const u32x4 v0 = *(const u32x4*)qp, v1 = *(const u32x4*)(qp + 8);
#pragma unroll
      for (int j = 0; j < 4; ++j) { q[2 * j] = lo16(v0[j]); q[2 * j + 1] = hi16(v0[j]); q[8 + 2 * j] = lo16(v1[j]); q[9 + 2 * j] = hi16(v1[j]); }
    }
    float lg[8];
    float mx = -3.0e38f;
#pragma unroll
    for (int w = 0; w < 8; ++w) {
      const int row = 16 + r - 2 * w;
      const u16* kp = smem + row * TLD + 64 + hl * 16;
      const u32x4 v0 = *(const u32x4*)kp, v1 = *(const u32x4*)(kp + 8);
      float d = 0.f;
#pragma unroll
      for (int j = 0; j < 4; ++j) {
        d += q[2 * j] * lo16(v0[j]); d += q[2 * j + 1] * hi16(v0[j]);
        d += q[8 + 2 * j] * lo16(v1[j]); d += q[9 + 2 * j] * hi16(v1[j]);
      }
      float l = rbf(rbf(d) * 0.25f);
      if (m0 + r - 2 * w < 0) l = rbf(-1000000000.0f);
      lg[w] = l;
      mx = fmaxf(mx, l);
    }
    float sum = 0.f;
#pragma unroll
    for (int w = 0; w < 8; ++w) { lg[w] = __expf(lg[w] - mx); sum += lg[w]; }
    const float inv = 1.f / sum;
    float ov[16];
#pragma unroll
    for (int j = 0; j < 16; ++j) ov[j] = 0.f;
#pragma unroll
    for (int w = 0; w < 8; ++w) {
      const int row = 16 + r - 2 * w;
      const float pw = rbf(lg[w] * inv);
      const u16* vp = smem + row * TLD + 128 + hl * 16;
      const u32x4 v0 = *(const u32x4*)vp, v1 = *(const u32x4*)(vp + 8);
#pragma unroll
      for (int j = 0; j < 4; ++j) {
        ov[2 * j] += pw * lo16(v0[j]); ov[2 * j + 1] += pw * hi16(v0[j]);
        ov[8 + 2 * j] += pw * lo16(v1[j]); ov[9 + 2 * j] += pw * hi16(v1[j]);
      }
    }
    u32x4 o0, o1;
#pragma unroll
    for (int j = 0; j < 4; ++j) { o0[j] = pack2(ov[2 * j], ov[2 * j + 1]); o1[j] = pack2(ov[8 + 2 * j], ov[9 + 2 * j]); }
    u16* op = o + (size_t)(m0 + r) * D_ + hg * 64 + hl * 16;
    *(u32x4*)op = o0;
    *(u32x4*)(op + 8) = o1;
  }
}

__global__ __launch_bounds__(512) void wo_kernel(const u16* __restrict__ o, const u16* __restrict__ wo,
                                                 const u16* __restrict__ x, const float* __restrict__ mod,
                                                 u16* __restrict__ x1, float2* __restrict__ stats) {
  constexpr int NSLOT = 8;
  __shared__ __attribute__((aligned(16))) u16 smem[NSLOT * 8192];
  const int tid = threadIdx.x, lane = tid & 63, wid = tid >> 6, wm = wid >> 1, wn = wid & 1;
  const int xcd = blockIdx.x & 7, idx = blockIdx.x >> 3;
  const int rb = xcd * 4 + (idx >> 3), cb = idx & 7;
  const int m0 = rb * 64, n0 = cb * 64;
  f32x4 acc[2][2];
#pragma unroll
  for (int a = 0; a < 2; ++a)
#pragma unroll
    for (int b = 0; b < 2; ++b) acc[a][b] = (f32x4){0.f, 0.f, 0.f, 0.f};
  u32x2 xv_pre[2][2]; float4 g_pre[2];
  if (tid < 256) {
#pragma unroll
    for (int mt = 0; mt < 2; ++mt)
#pragma unroll
      for (int nt = 0; nt < 2; ++nt)
        xv_pre[mt][nt] = *(const u32x2*)(x + (size_t)(m0 + wm * 32 + mt * 16 + (lane & 15)) * D_ + n0 + wn * 32 + nt * 16 + (lane >> 4) * 4);
#pragma unroll
    for (int nt = 0; nt < 2; ++nt) g_pre[nt] = *(const float4*)(mod + 2 * D_ + n0 + wn * 32 + nt * 16 + (lane >> 4) * 4);
  }
  gemm64_dma<D_, NSLOT>(o + (size_t)m0 * D_, D_, wo + n0, D_, smem, acc, tid);
  __syncthreads();
  float* T = (float*)smem;
  if (tid < 256)
#pragma unroll
  for (int mt = 0; mt < 2; ++mt)
#pragma unroll
    for (int nt = 0; nt < 2; ++nt) {
      const int m = wm * 32 + mt * 16 + (lane & 15), n = wn * 32 + nt * 16 + (lane >> 4) * 4;
      const u32x2 xv = xv_pre[mt][nt];
      float r[4];
      const float4 gav = g_pre[nt];
      r[0] = rbf(lo16(xv[0]) + rbf(gav.x * rbf(acc[mt][nt][0])));
      r[1] = rbf(hi16(xv[0]) + rbf(gav.y * rbf(acc[mt][nt][1])));
      r[2] = rbf(lo16(xv[1]) + rbf(gav.z * rbf(acc[mt][nt][2])));
      r[3] = rbf(hi16(xv[1]) + rbf(gav.w * rbf(acc[mt][nt][3])));
      u32x2 w; w[0] = pack2(r[0], r[1]); w[1] = pack2(r[2], r[3]);
      *(u32x2*)(x1 + (size_t)(m0 + m) * D_ + n0 + n) = w;
#pragma unroll
      for (int j = 0; j < 4; ++j) T[m * 65 + n + j] = r[j];
    }
  __syncthreads();
  if (tid < 64) {
    float s = 0.f;
#pragma unroll 16
    for (int j = 0; j < 64; ++j) s += T[tid * 65 + j];
    const float mean = s * (1.f / 64.f);
    float q = 0.f;
#pragma unroll 16
    for (int j = 0; j < 64; ++j) { const float d = T[tid * 65 + j] - mean; q += d * d; }
    stats[(size_t)(m0 + tid) * 8 + cb] = make_float2(mean, q);
  }
}

__global__ __launch_bounds__(256) void ln2_kernel(const u16* __restrict__ x1, const float* __restrict__ mod,
                                                  u16* __restrict__ h2) {
  const int tid = threadIdx.x, wave = tid >> 6, lane = tid & 63;
  const int row = blockIdx.x * 4 + wave;
  const u32x4 v = *(const u32x4*)(x1 + (size_t)row * D_ + lane * 8);
  const float4 s0 = *(const float4*)(mod + 3 * D_ + lane * 8), s1 = *(const float4*)(mod + 3 * D_ + lane * 8 + 4);
  const float4 c0 = *(const float4*)(mod + 4 * D_ + lane * 8), c1 = *(const float4*)(mod + 4 * D_ + lane * 8 + 4);
  const float sb[8] = {s0.x, s0.y, s0.z, s0.w, s1.x, s1.y, s1.z, s1.w};
  const float cb[8] = {c0.x, c0.y, c0.z, c0.w, c1.x, c1.y, c1.z, c1.w};
  float f[8];
#pragma unroll
  for (int j = 0; j < 4; ++j) { f[2 * j] = lo16(v[j]); f[2 * j + 1] = hi16(v[j]); }
  float sm = 0.f;
#pragma unroll
  for (int j = 0; j < 8; ++j) sm += f[j];
#pragma unroll
  for (int o = 32; o >= 1; o >>= 1) sm += __shfl_xor(sm, o);
  const float mean = sm * (1.f / D_);
  float q = 0.f;
#pragma unroll
  for (int j = 0; j < 8; ++j) { const float d = f[j] - mean; q += d * d; }
#pragma unroll
  for (int o = 32; o >= 1; o >>= 1) q += __shfl_xor(q, o);
  const float var = rbf(q * (1.f / D_));
  const float mb = rbf(mean);
  const float den = rbf(sqrtf(rbf(var + 1e-5f)));
  float h[8];
#pragma unroll
  for (int j = 0; j < 8; ++j) h[j] = rbf(rbf(rbf(f[j] - mb) / den) * rbf(1.f + cb[j])) + sb[j];
  u32x4 o;
#pragma unroll
  for (int j = 0; j < 4; ++j) o[j] = pack2(h[2 * j], h[2 * j + 1]);
  *(u32x4*)(h2 + (size_t)row * D_ + lane * 8) = o;
}

__global__ __launch_bounds__(768) void gateup_kernel(const u16* __restrict__ h2, const u16* __restrict__ wg,
                                                     const u16* __restrict__ wu, u16* __restrict__ hh) {
  constexpr int NSLOT = 3, SLOT = 8192 + 4 * 4096, NK = D_ / 64;
  __shared__ __attribute__((aligned(16))) u16 smem[NSLOT * SLOT];
  const int tid = threadIdx.x, lane = tid & 63, wid = __builtin_amdgcn_readfirstlane(tid >> 6);
  const int xcd = blockIdx.x & 7, idx = blockIdx.x >> 3;
  const int rb = (xcd >> 1) * 4 + idx / 7, cbk = (xcd & 1) * 7 + idx % 7;
  const int m0 = rb * 128, f0 = cbk * 128;
  if (wid >= 8) {
    const int lw = wid - 8;
    const u16* src = ((lw >> 1) ? wu : wg) + f0 + (lw & 1) * 64;
    const u16* asrc = h2 + (size_t)m0 * D_;
    int boff[8], aoff[4];
#pragma unroll
    for (int i = 0; i < 8; ++i) {
      const int sidx = i * 64 + lane, row = sidx >> 3, pos = sidx & 7;
      boff[i] = row * DF_ + ((pos ^ swzB(row)) * 8);
    }
#pragma unroll
    for (int i = 0; i < 4; ++i) {
      const int sidx = (lw * 4 + i) * 64 + lane, row = sidx >> 3, pos = sidx & 7;
      aoff[i] = row * D_ + ((pos ^ swzA(row)) * 8);
    }
    auto issue = [&](int kt) {
      u16* st = smem + (kt % NSLOT) * SLOT;
#pragma unroll
      for (int i = 0; i < 4; ++i) glds16(asrc + kt * 64 + aoff[i], st + (lw * 4 + i) * 512);
#pragma unroll
      for (int i = 0; i < 8; ++i) glds16(src + (size_t)kt * 64 * DF_ + boff[i], st + 8192 + lw * 4096 + i * 512);
    };
    issue(0); issue(1);
    for (int kt = 0; kt < NK; ++kt) {
      if (kt + 1 < NK) wait_vm<12>(); else wait_vm<0>();
      raw_barrier();
      if (kt + 2 < NK) issue(kt + 2);
    }
  } else {
    const int wm = wid >> 2, wn = wid & 3;
    f32x4 accg[4][2], accu[4][2];
#pragma unroll
    for (int a = 0; a < 4; ++a)
#pragma unroll
      for (int b = 0; b < 2; ++b) { accg[a][b] = (f32x4){0.f, 0.f, 0.f, 0.f}; accu[a][b] = (f32x4){0.f, 0.f, 0.f, 0.f}; }
    const int ntg = (wn >> 1) * 4 + (wn & 1) * 2;
    for (int kt = 0; kt < NK; ++kt) {
      raw_barrier();
      const u16* st = smem + (kt % NSLOT) * SLOT;
      mma64<4, 2>(st, st + 8192, accg, wm * 4, ntg, lane);
      mma64<4, 2>(st, st + 8192, accu, wm * 4, 8 + ntg, lane);
    }
#pragma unroll
    for (int mt = 0; mt < 4; ++mt)
#pragma unroll
      for (int nt = 0; nt < 2; ++nt) {
        const int m = wm * 64 + mt * 16 + (lane & 15), n = wn * 32 + nt * 16 + (lane >> 4) * 4;
        float r[4];
#pragma unroll
        for (int j = 0; j < 4; ++j) {
          const float g = rbf(accg[mt][nt][j]), u = rbf(accu[mt][nt][j]);
          const float y = 0.7978845608028654f * (g + 0.044715f * g * g * g);
          const float th = 1.f - 2.f / (__expf(2.f * y) + 1.f);
          const float ge = rbf(0.5f * g * (1.f + th));
          r[j] = ge * u;
        }
        u32x2 w; w[0] = pack2(r[0], r[1]); w[1] = pack2(r[2], r[3]);
        *(u32x2*)(hh + (size_t)(m0 + m) * DF_ + f0 + n) = w;
      }
  }
}

__global__ __launch_bounds__(512) void down_kernel(const u16* __restrict__ hh, const u16* __restrict__ wd,
                                                   const u16* __restrict__ x1, const float* __restrict__ mod,
                                                   u16* __restrict__ out) {
  constexpr int NSLOT = 8;
  __shared__ __attribute__((aligned(16))) u16 smem[NSLOT * 8192];
  const int tid = threadIdx.x, lane = tid & 63, wid = tid >> 6, wm = wid >> 1, wn = wid & 1;
  const int xcd = blockIdx.x & 7, idx = blockIdx.x >> 3;
  const int rb = xcd * 4 + (idx >> 3), cb = idx & 7;
  const int m0 = rb * 64, n0 = cb * 64;
  f32x4 acc[2][2];
#pragma unroll
  for (int a = 0; a < 2; ++a)
#pragma unroll
    for (int b = 0; b < 2; ++b) acc[a][b] = (f32x4){0.f, 0.f, 0.f, 0.f};
  u32x2 xv_pre[2][2]; float4 g_pre[2];
  if (tid < 256) {
#pragma unroll
    for (int mt = 0; mt < 2; ++mt)
#pragma unroll
      for (int nt = 0; nt < 2; ++nt)
        xv_pre[mt][nt] = *(const u32x2*)(x1 + (size_t)(m0 + wm * 32 + mt * 16 + (lane & 15)) * D_ + n0 + wn * 32 + nt * 16 + (lane >> 4) * 4);
#pragma unroll
    for (int nt = 0; nt < 2; ++nt) g_pre[nt] = *(const float4*)(mod + 5 * D_ + n0 + wn * 32 + nt * 16 + (lane >> 4) * 4);
  }
  gemm64_dma<DF_, NSLOT>(hh + (size_t)m0 * DF_, DF_, wd + n0, D_, smem, acc, tid);
  if (tid < 256)
#pragma unroll
  for (int mt = 0; mt < 2; ++mt)
#pragma unroll
    for (int nt = 0; nt < 2; ++nt) {
      const int m = wm * 32 + mt * 16 + (lane & 15), n = wn * 32 + nt * 16 + (lane >> 4) * 4;
      const u32x2 xv = xv_pre[mt][nt];
      const float4 gbv = g_pre[nt];
      u32x2 w;
      w[0] = pack2(lo16(xv[0]) + rbf(gbv.x * rbf(acc[mt][nt][0])), hi16(xv[0]) + rbf(gbv.y * rbf(acc[mt][nt][1])));
      w[1] = pack2(lo16(xv[1]) + rbf(gbv.z * rbf(acc[mt][nt][2])), hi16(xv[1]) + rbf(gbv.w * rbf(acc[mt][nt][3])));
      *(u32x2*)(out + (size_t)(m0 + m) * D_ + n0 + n) = w;
    }
}

extern "C" void kernel_launch(void* const* d_in, const int* in_sizes, int n_in, void* d_out, int out_size,
                              void* d_ws, size_t ws_size, hipStream_t stream) {
  const u16* x = (const u16*)d_in[0];
  const u16* wqkv = (const u16*)d_in[1];
  const u16* wo = (const u16*)d_in[2];
  const u16* wg = (const u16*)d_in[4];
  const u16* wu = (const u16*)d_in[5];
  const u16* wd = (const u16*)d_in[6];
  const u16* c = (const u16*)d_in[7];
  const u16* wmod = (const u16*)d_in[8];
  u16* out = (u16*)d_out;
  char* ws = (char*)d_ws;
  float* mod = (float*)ws;
  float2* stats = (float2*)(ws + 16384);
  u16* xn = (u16*)(ws + 16384 + 131072);
  u16* o = xn + (size_t)S_ * D_;
  u16* x1 = o + (size_t)S_ * D_;
  u16* hh = x1 + (size_t)S_ * D_;

  prep_kernel<<<PREP_MOD_BLOCKS + 256, 256, 0, stream>>>(x, c, wmod, mod, xn);
  qkv_attn_kernel<<<256, 256, 0, stream>>>(xn, wqkv, mod, o);
  wo_kernel<<<256, 512, 0, stream>>>(o, wo, x, mod, x1, stats);
  ln2_kernel<<<S_ / 4, 256, 0, stream>>>(x1, mod, xn);
  gateup_kernel<<<16 * 14, 768, 0, stream>>>(xn, wg, wu, hh);
  down_kernel<<<256, 512, 0, stream>>>(hh, wd, x1, mod, out);
}
```

```cpp
#include <hip/hip_runtime.h>
#include <hip/hip_bf16.h>
#include <stdint.h>


typedef unsigned short u16;
typedef short bf16x8 __attribute__((ext_vector_type(8)));
typedef short bf16x4 __attribute__((ext_vector_type(4)));
typedef float f32x4 __attribute__((ext_vector_type(4)));
typedef unsigned u32x4 __attribute__((ext_vector_type(4)));
typedef unsigned u32x2 __attribute__((ext_vector_type(2)));
typedef __attribute__((address_space(3))) bf16x4 lds_bf16x4;

constexpr int S_ = 2048, D_ = 512, DF_ = 1792;

__device__ __forceinline__ float bf2f(u16 v) { return __uint_as_float(((unsigned)v) << 16); }
__device__ __forceinline__ u16 f2bf(float f) { __bf16 b = (__bf16)f; return __builtin_bit_cast(u16, b); }
__device__ __forceinline__ float rbf(float f) { return bf2f(f2bf(f)); }
__device__ __forceinline__ float lo16(unsigned u) { return __uint_as_float(u << 16); }
__device__ __forceinline__ float hi16(unsigned u) { return __uint_as_float(u & 0xffff0000u); }
__device__ __forceinline__ unsigned pack2(float a, float b) { return (unsigned)f2bf(a) | ((unsigned)f2bf(b) << 16); }

__device__ __forceinline__ bf16x4 tr_read(const u16* p) {
  return __builtin_amdgcn_ds_read_tr16_b64_v4i16((lds_bf16x4*)p);
}


typedef __attribute__((address_space(3))) void lds_void;
__device__ __forceinline__ void glds16(const u16* g, u16* l) {
  __builtin_amdgcn_global_load_lds((const void*)g, (lds_void*)l, 16, 0, 0);
}
template <int N> __device__ __forceinline__ void wait_vm() { asm volatile("s_waitcnt vmcnt(%0)" ::"n"(N) : "memory"); }
__device__ __forceinline__ void raw_barrier() {
  asm volatile("s_waitcnt lgkmcnt(0)" ::: "memory");
  __builtin_amdgcn_s_barrier();
  asm volatile("" ::: "memory");
}
__device__ __forceinline__ int swzA(int row) { return row & 7; }
__device__ __forceinline__ int swzB(int row) { return (((row >> 1) & 1) << 1) | (((row >> 3) & 1) << 2); }

template <int MT, int NTL>
__device__ __forceinline__ void mma64(const u16* __restrict__ As, const u16* __restrict__ Bs, f32x4 (&acc)[MT][NTL],
                                      int mt0, int nt0, int lane) {
  const int g = lane >> 4, q = (lane & 15) >> 2, p = lane & 3;
#pragma unroll
  for (int ks = 0; ks < 2; ++ks) {
    bf16x8 a[MT], w[NTL];
#pragma unroll
    for (int mt = 0; mt < MT; ++mt) {
      const int row = (mt0 + mt) * 16 + (lane & 15);
      a[mt] = *(const bf16x8*)(As + row * 64 + (((ks * 4 + g) ^ swzA(row)) * 8));
    }
#pragma unroll
    for (int nt = 0; nt < NTL; ++nt) {
      const int ntg = nt0 + nt;
      const u16* bimg = Bs + (ntg >> 2) * 4096;
      const int r0 = ks * 32 + 8 * g + q, r1 = r0 + 4;
      const int ch = (ntg & 3) * 2 + (p >> 1);
      const bf16x4 t0 = tr_read(bimg + r0 * 64 + ((ch ^ swzB(r0)) * 8) + (p & 1) * 4);
      const bf16x4 t1 = tr_read(bimg + r1 * 64 + ((ch ^ swzB(r1)) * 8) + (p & 1) * 4);
      w[nt] = __builtin_shufflevector(t0, t1, 0, 1, 2, 3, 4, 5, 6, 7);
    }
#pragma unroll
    for (int mt = 0; mt < MT; ++mt)
#pragma unroll
      for (int nt = 0; nt < NTL; ++nt)
        acc[mt][nt] = __builtin_amdgcn_mfma_f32_16x16x32_bf16(w[nt], a[mt], acc[mt][nt], 0, 0, 0);
  }
}

__device__ __forceinline__ void mma64_22(const u16* __restrict__ As, const u16* __restrict__ Bs, f32x4 (&acc)[2][2],
                                         int mt0, int nt0, int lane) {
  const int g = lane >> 4, q = (lane & 15) >> 2, p = lane & 3;
  bf16x8 a[2][2], w[2][2];
#pragma unroll
  for (int ks = 0; ks < 2; ++ks) {
#pragma unroll
    for (int mt = 0; mt < 2; ++mt) {
      const int row = (mt0 + mt) * 16 + (lane & 15);
      a[ks][mt] = *(const bf16x8*)(As + row * 64 + (((ks * 4 + g) ^ swzA(row)) * 8));
    }
#pragma unroll
    for (int nt = 0; nt < 2; ++nt) {
      const int r0 = ks * 32 + 8 * g + q, r1 = r0 + 4;
      const int ch = (nt0 + nt) * 2 + (p >> 1);
      const bf16x4 t0 = tr_read(Bs + r0 * 64 + ((ch ^ swzB(r0)) * 8) + (p & 1) * 4);
      const bf16x4 t1 = tr_read(Bs + r1 * 64 + ((ch ^ swzB(r1)) * 8) + (p & 1) * 4);
      w[ks][nt] = __builtin_shufflevector(t0, t1, 0, 1, 2, 3, 4, 5, 6, 7);
    }
  }
#pragma unroll
  for (int ks = 0; ks < 2; ++ks)
#pragma unroll
    for (int mt = 0; mt < 2; ++mt)
#pragma unroll
      for (int nt = 0; nt < 2; ++nt)
        acc[mt][nt] = __builtin_amdgcn_mfma_f32_16x16x32_bf16(w[ks][nt], a[ks][mt], acc[mt][nt], 0, 0, 0);
}

template <int K, int NSLOT>
__device__ __forceinline__ void gemm64_dma(const u16* __restrict__ A, int lda, const u16* __restrict__ B, int ldb,
                                           u16* smem, f32x4 (&acc)[2][2], int tid) {
  constexpr int NK = K / 64, DEPTH = NSLOT - 1;
  const int lane = tid & 63, wid = __builtin_amdgcn_readfirstlane(tid >> 6);
  if (wid >= 4) {
    const int lw = wid - 4;
    int aoff[2], boff[2];
#pragma unroll
    for (int i = 0; i < 2; ++i) {
      const int s = (i * 4 + lw) * 64 + lane, row = s >> 3, pos = s & 7;
      aoff[i] = row * lda + ((pos ^ swzA(row)) * 8);
      boff[i] = row * ldb + ((pos ^ swzB(row)) * 8);
    }
    auto issue = [&](int kt) {
      u16* st = smem + (kt % NSLOT) * 8192;
#pragma unroll
      for (int i = 0; i < 2; ++i) glds16(A + kt * 64 + aoff[i], st + (i * 4 + lw) * 512);
#pragma unroll
      for (int i = 0; i < 2; ++i) glds16(B + (size_t)kt * 64 * ldb + boff[i], st + 4096 + (i * 4 + lw) * 512);
    };
#pragma unroll
    for (int s = 0; s < DEPTH; ++s) issue(s);
    for (int kt = 0; kt < NK - DEPTH + 1; ++kt) {
      wait_vm<(DEPTH - 1) * 4>();
      raw_barrier();
      if (kt + DEPTH < NK) issue(kt + DEPTH);
    }
#pragma unroll
    for (int kt = NK - DEPTH + 1; kt < NK; ++kt) {
      wait_vm<0>();
      raw_barrier();
    }
  } else {
    const int wm = wid >> 1, wn = wid & 1;
    for (int kt = 0; kt < NK; ++kt) {
      raw_barrier();
      const u16* st = smem + (kt % NSLOT) * 8192;
      mma64_22(st, st + 4096, acc, wm * 2, wn * 2, lane);
    }
  }
}

constexpr int PREP_MOD_BLOCKS = 96;
__global__ __launch_bounds__(256) void prep_kernel(const u16* __restrict__ x, const u16* __restrict__ c,
                                                   const u16* __restrict__ wmod, float* __restrict__ mod,
                                                   u16* __restrict__ xn) {
  __shared__ float red[64][33];
  const int tid = threadIdx.x;
  if (blockIdx.x < PREP_MOD_BLOCKS) {
    const int n0 = blockIdx.x * 32;
    const int cg = tid & 3, kl = tid >> 2;
    float acc[8];
#pragma unroll
    for (int j = 0; j < 8; ++j) acc[j] = 0.f;
#pragma unroll
    for (int i = 0; i < 8; ++i) {
      const int k = kl + 64 * i;
      const float cv = bf2f(c[k]);
      const float sv = rbf(cv / (1.f + __expf(-cv)));
      const u32x4 w = *(const u32x4*)(wmod + (size_t)k * 3072 + n0 + cg * 8);
#pragma unroll
      for (int j = 0; j < 4; ++j) {
        acc[2 * j] += sv * lo16(w[j]);
        acc[2 * j + 1] += sv * hi16(w[j]);
      }
    }
#pragma unroll
    for (int j = 0; j < 8; ++j) red[kl][cg * 8 + j] = acc[j];
    __syncthreads();
    if (tid < 32) {
      float s = 0.f;
#pragma unroll 8
      for (int i = 0; i < 64; ++i) s += red[i][tid];
      mod[n0 + tid] = rbf(s);
    }
  } else {
    const int b = blockIdx.x - PREP_MOD_BLOCKS;
    const int wave = tid >> 6, lane = tid & 63;
#pragma unroll
    for (int rr = 0; rr < 2; ++rr) {
      const int row = b * 8 + wave * 2 + rr;
      const u32x4 v = *(const u32x4*)(x + (size_t)row * D_ + lane * 8);
      float f[8];
#pragma unroll
      for (int j = 0; j < 4; ++j) { f[2 * j] = lo16(v[j]); f[2 * j + 1] = hi16(v[j]); }
      float s = 0.f;
#pragma unroll
      for (int j = 0; j < 8; ++j) s += f[j];
#pragma unroll
      for (int o = 32; o >= 1; o >>= 1) s += __shfl_xor(s, o);
      const float mean = s * (1.f / D_);
      float q = 0.f;
#pragma unroll
      for (int j = 0; j < 8; ++j) { const float d = f[j] - mean; q += d * d; }
#pragma unroll
      for (int o = 32; o >= 1; o >>= 1) q += __shfl_xor(q, o);
      const float var = rbf(q * (1.f / D_));
      const float mb = rbf(mean);
      const float den = rbf(sqrtf(rbf(var + 1e-5f)));
      u32x4 o;
#pragma unroll
      for (int j = 0; j < 4; ++j) o[j] = pack2(rbf(f[2 * j] - mb) / den, rbf(f[2 * j + 1] - mb) / den);
      *(u32x4*)(xn + (size_t)row * D_ + lane * 8) = o;
    }
  }
}

template <int BM, int BN, int WM, int WN>
struct Core {
  static constexpr int BK = 64;
  static constexpr int NT = WM * WN * 64;
  static constexpr int LDA = BK + 8;
  static constexpr int LDB = BN + 16;
  static constexpr int A_ELEMS = BM * LDA, B_ELEMS = BK * LDB;
  static constexpr int STAGE_ELEMS = A_ELEMS + B_ELEMS;
  static constexpr int ACH = BM * (BK / 8), BCH = BK * (BN / 8);
  static constexpr int AIT = (ACH + NT - 1) / NT, BIT = (BCH + NT - 1) / NT;
  static constexpr int MT = BM / WM / 16, NTL = BN / WN / 16;
};

template <int BM, int BN, int WM, int WN>
__device__ __forceinline__ void mma_step(const u16* __restrict__ As, const u16* __restrict__ Bs,
                                         f32x4 (&acc)[Core<BM, BN, WM, WN>::MT][Core<BM, BN, WM, WN>::NTL],
                                         int wm, int wn, int lane) {
  using C = Core<BM, BN, WM, WN>;
  const int g = lane >> 4, q = (lane & 15) >> 2, p = lane & 3;
#pragma unroll
  for (int ks = 0; ks < 2; ++ks) {
    bf16x8 a[C::MT], w[C::NTL];
#pragma unroll
    for (int mt = 0; mt < C::MT; ++mt)
      a[mt] = *(const bf16x8*)(As + (wm * (BM / WM) + mt * 16 + (lane & 15)) * C::LDA + ks * 32 + g * 8);
#pragma unroll
    for (int nt = 0; nt < C::NTL; ++nt) {
      const u16* bp = Bs + (ks * 32 + 8 * g + q) * C::LDB + wn * (BN / WN) + nt * 16 + 4 * p;
      const bf16x4 t0 = tr_read(bp), t1 = tr_read(bp + 4 * C::LDB);
      w[nt] = __builtin_shufflevector(t0, t1, 0, 1, 2, 3, 4, 5, 6, 7);
    }
#pragma unroll
    for (int mt = 0; mt < C::MT; ++mt)
#pragma unroll
      for (int nt = 0; nt < C::NTL; ++nt)
        acc[mt][nt] = __builtin_amdgcn_mfma_f32_16x16x32_bf16(w[nt], a[mt], acc[mt][nt], 0, 0, 0);
  }
}

__global__ __launch_bounds__(512) void qkv_attn_kernel(const u16* __restrict__ h1, const u16* __restrict__ wqkv,
                                                       u16* __restrict__ o) {
  constexpr int NSLOT = 4, SLOT = 5120 + 3 * 4096, NK = D_ / 64, DEPTH = NSLOT - 1;
  constexpr int TLD = 200;
  __shared__ __attribute__((aligned(16))) u16 smem[NSLOT * SLOT];
  const int tid = threadIdx.x, lane = tid & 63, wid = __builtin_amdgcn_readfirstlane(tid >> 6), wn = wid & 3;
  const int xcd = blockIdx.x & 7, idx = blockIdx.x >> 3;
  const int rb = (xcd >> 1) * 8 + (idx >> 2), hg = (xcd & 1) * 4 + (idx & 3);
  const int m0 = rb * 64;
  f32x4 acc[5][3];
#pragma unroll
  for (int a = 0; a < 5; ++a)
#pragma unroll
    for (int b = 0; b < 3; ++b) acc[a][b] = (f32x4){0.f, 0.f, 0.f, 0.f};
  if (wid >= 4) {
    const int lw = wid - 4;
    int aoff[3], boff[6];
#pragma unroll
    for (int i = 0; i < 3; ++i) {
      const int pc = lw + 4 * i, sidx = pc * 64 + lane, row = sidx >> 3, pos = sidx & 7;
      int grow = m0 - 16 + row; grow = grow < 0 ? 0 : grow;
      aoff[i] = grow * D_ + ((pos ^ swzA(row)) * 8);
    }
#pragma unroll
    for (int i = 0; i < 6; ++i) {
      const int pc = lw * 6 + i, img = pc >> 3, sidx = (pc & 7) * 64 + lane, row = sidx >> 3, pos = sidx & 7;
      boff[i] = row * 1536 + img * 512 + hg * 64 + ((pos ^ swzB(row)) * 8);
    }
    auto issue = [&](int kt) {
      u16* st = smem + (kt % NSLOT) * SLOT;
#pragma unroll
      for (int i = 0; i < 3; ++i)
        if (lw + 4 * i < 10) glds16(h1 + kt * 64 + aoff[i], st + (lw + 4 * i) * 512);
#pragma unroll
      for (int i = 0; i < 6; ++i) glds16(wqkv + (size_t)kt * 64 * 1536 + boff[i], st + 5120 + (lw * 6 + i) * 512);
    };
#pragma unroll
    for (int st = 0; st < DEPTH; ++st) issue(st);
    for (int kt = 0; kt < NK - DEPTH + 1; ++kt) {
      wait_vm<(DEPTH - 1) * 8>();
      raw_barrier();
      if (kt + DEPTH < NK) issue(kt + DEPTH);
    }
#pragma unroll
    for (int kt = NK - DEPTH + 1; kt < NK; ++kt) { wait_vm<0>(); raw_barrier(); }
  } else {
    for (int kt = 0; kt < NK; ++kt) {
      raw_barrier();
      const u16* st = smem + (kt % NSLOT) * SLOT;
      mma64<5, 3>(st, st + 5120, acc, 0, wn * 3, lane);
    }
  }
  __syncthreads();
  using C = Core<80, 192, 1, 4>;
  if (tid < 256)
#pragma unroll
  for (int mt = 0; mt < C::MT; ++mt)
#pragma unroll
    for (int nt = 0; nt < C::NTL; ++nt) {
      const int m = mt * 16 + (lane & 15), n = wn * 48 + nt * 16 + (lane >> 4) * 4;
      u32x2 w;
      w[0] = pack2(acc[mt][nt][0], acc[mt][nt][1]);
      w[1] = pack2(acc[mt][nt][2], acc[mt][nt][3]);
      *(u32x2*)(smem + m * TLD + n) = w;
    }
  __syncthreads();
  if (tid < 256) {
    const int r = tid >> 2, hl = tid & 3;
    const u16* qp = smem + (16 + r) * TLD + hl * 16;
    float q[16];
    {
      const u32x4 v0 = *(const u32x4*)qp, v1 = *(const u32x4*)(qp + 8);
#pragma unroll
      for (int j = 0; j < 4; ++j) { q[2 * j] = lo16(v0[j]); q[2 * j + 1] = hi16(v0[j]); q[8 + 2 * j] = lo16(v1[j]); q[9 + 2 * j] = hi16(v1[j]); }
    }
    float lg[8];
    float mx = -3.0e38f;
#pragma unroll
    for (int w = 0; w < 8; ++w) {
      const int row = 16 + r - 2 * w;
      const u16* kp = smem + row * TLD + 64 + hl * 16;
      const u32x4 v0 = *(const u32x4*)kp, v1 = *(const u32x4*)(kp + 8);
      float d = 0.f;
#pragma unroll
      for (int j = 0; j < 4; ++j) {
        d += q[2 * j] * lo16(v0[j]); d += q[2 * j + 1] * hi16(v0[j]);
        d += q[8 + 2 * j] * lo16(v1[j]); d += q[9 + 2 * j] * hi16(v1[j]);
      }
      float l = rbf(rbf(d) * 0.25f);
      if (m0 + r - 2 * w < 0) l = rbf(-1000000000.0f);
      lg[w] = l;
      mx = fmaxf(mx, l);
    }
    float sum = 0.f;
#pragma unroll
    for (int w = 0; w < 8; ++w) { lg[w] = __expf(lg[w] - mx); sum += lg[w]; }
    const float inv = 1.f / sum;
    float ov[16];
#pragma unroll
    for (int j = 0; j < 16; ++j) ov[j] = 0.f;
#pragma unroll
    for (int w = 0; w < 8; ++w) {
      const int row = 16 + r - 2 * w;
      const float pw = rbf(lg[w] * inv);
      const u16* vp = smem + row * TLD + 128 + hl * 16;
      const u32x4 v0 = *(const u32x4*)vp, v1 = *(const u32x4*)(vp + 8);
#pragma unroll
      for (int j = 0; j < 4; ++j) {
        ov[2 * j] += pw * lo16(v0[j]); ov[2 * j + 1] += pw * hi16(v0[j]);
        ov[8 + 2 * j] += pw * lo16(v1[j]); ov[9 + 2 * j] += pw * hi16(v1[j]);
      }
    }
    u32x4 o0, o1;
#pragma unroll
    for (int j = 0; j < 4; ++j) { o0[j] = pack2(ov[2 * j], ov[2 * j + 1]); o1[j] = pack2(ov[8 + 2 * j], ov[9 + 2 * j]); }
    u16* op = o + (size_t)(m0 + r) * D_ + hg * 64 + hl * 16;
    *(u32x4*)op = o0;
    *(u32x4*)(op + 8) = o1;
  }
}

__global__ __launch_bounds__(512) void wo_kernel(const u16* __restrict__ o, const u16* __restrict__ wo,
                                                 const u16* __restrict__ x, const float* __restrict__ mod,
                                                 u16* __restrict__ x1, float2* __restrict__ stats) {
  constexpr int NSLOT = 8;
  __shared__ __attribute__((aligned(16))) u16 smem[NSLOT * 8192];
  const int tid = threadIdx.x, lane = tid & 63, wid = tid >> 6, wm = wid >> 1, wn = wid & 1;
  const int xcd = blockIdx.x & 7, idx = blockIdx.x >> 3;
  const int rb = xcd * 4 + (idx >> 3), cb = idx & 7;
  const int m0 = rb * 64, n0 = cb * 64;
  f32x4 acc[2][2];
#pragma unroll
  for (int a = 0; a < 2; ++a)
#pragma unroll
    for (int b = 0; b < 2; ++b) acc[a][b] = (f32x4){0.f, 0.f, 0.f, 0.f};
  u32x2 xv_pre[2][2]; float4 g_pre[2];
  if (tid < 256) {
#pragma unroll
    for (int mt = 0; mt < 2; ++mt)
#pragma unroll
      for (int nt = 0; nt < 2; ++nt)
        xv_pre[mt][nt] = *(const u32x2*)(x + (size_t)(m0 + wm * 32 + mt * 16 + (lane & 15)) * D_ + n0 + wn * 32 + nt * 16 + (lane >> 4) * 4);
#pragma unroll
    for (int nt = 0; nt < 2; ++nt) g_pre[nt] = *(const float4*)(mod + 2 * D_ + n0 + wn * 32 + nt * 16 + (lane >> 4) * 4);
  }
  gemm64_dma<D_, NSLOT>(o + (size_t)m0 * D_, D_, wo + n0, D_, smem, acc, tid);
  __syncthreads();
  float* T = (float*)smem;
  if (tid < 256)
#pragma unroll
  for (int mt = 0; mt < 2; ++mt)
#pragma unroll
    for (int nt = 0; nt < 2; ++nt) {
      const int m = wm * 32 + mt * 16 + (lane & 15), n = wn * 32 + nt * 16 + (lane >> 4) * 4;
      const u32x2 xv = xv_pre[mt][nt];
      float r[4];
      const float4 gav = g_pre[nt];
      r[0] = rbf(lo16(xv[0]) + rbf(gav.x * rbf(acc[mt][nt][0])));
      r[1] = rbf(hi16(xv[0]) + rbf(gav.y * rbf(acc[mt][nt][1])));
      r[2] = rbf(lo16(xv[1]) + rbf(gav.z * rbf(acc[mt][nt][2])));
      r[3] = rbf(hi16(xv[1]) + rbf(gav.w * rbf(acc[mt][nt][3])));
      u32x2 w; w[0] = pack2(r[0], r[1]); w[1] = pack2(r[2], r[3]);
      *(u32x2*)(x1 + (size_t)(m0 + m) * D_ + n0 + n) = w;
#pragma unroll
      for (int j = 0; j < 4; ++j) T[m * 65 + n + j] = r[j];
    }
  __syncthreads();
  if (tid < 64) {
    float s = 0.f;
#pragma unroll 16
    for (int j = 0; j < 64; ++j) s += T[tid * 65 + j];
    const float mean = s * (1.f / 64.f);
    float q = 0.f;
#pragma unroll 16
    for (int j = 0; j < 64; ++j) { const float d = T[tid * 65 + j] - mean; q += d * d; }
    stats[(size_t)(m0 + tid) * 8 + cb] = make_float2(mean, q);
  }
}

__global__ __launch_bounds__(256) void ln2_kernel(const u16* __restrict__ x1, const float* __restrict__ mod,
                                                  u16* __restrict__ h2, int shift_off, int scale_off) {
  const int tid = threadIdx.x, wave = tid >> 6, lane = tid & 63;
  const int row = blockIdx.x * 4 + wave;
  const u32x4 v = *(const u32x4*)(x1 + (size_t)row * D_ + lane * 8);
  const float4 s0 = *(const float4*)(mod + shift_off + lane * 8), s1 = *(const float4*)(mod + shift_off + lane * 8 + 4);
  const float4 c0 = *(const float4*)(mod + scale_off + lane * 8), c1 = *(const float4*)(mod + scale_off + lane * 8 + 4);
  const float sb[8] = {s0.x, s0.y, s0.z, s0.w, s1.x, s1.y, s1.z, s1.w};
  const float cb[8] = {c0.x, c0.y, c0.z, c0.w, c1.x, c1.y, c1.z, c1.w};
  float f[8];
#pragma unroll
  for (int j = 0; j < 4; ++j) { f[2 * j] = lo16(v[j]); f[2 * j + 1] = hi16(v[j]); }
  float sm = 0.f;
#pragma unroll
  for (int j = 0; j < 8; ++j) sm += f[j];
#pragma unroll
  for (int o = 32; o >= 1; o >>= 1) sm += __shfl_xor(sm, o);
  const float mean = sm * (1.f / D_);
  float q = 0.f;
#pragma unroll
  for (int j = 0; j < 8; ++j) { const float d = f[j] - mean; q += d * d; }
#pragma unroll
  for (int o = 32; o >= 1; o >>= 1) q += __shfl_xor(q, o);
  const float var = rbf(q * (1.f / D_));
  const float mb = rbf(mean);
  const float den = rbf(sqrtf(rbf(var + 1e-5f)));
  float h[8];
#pragma unroll
  for (int j = 0; j < 8; ++j) h[j] = rbf(rbf(rbf(f[j] - mb) / den) * rbf(1.f + cb[j])) + sb[j];
  u32x4 o;
#pragma unroll
  for (int j = 0; j < 4; ++j) o[j] = pack2(h[2 * j], h[2 * j + 1]);
  *(u32x4*)(h2 + (size_t)row * D_ + lane * 8) = o;
}

__global__ __launch_bounds__(768) void gateup_kernel(const u16* __restrict__ h2, const u16* __restrict__ wg,
                                                     const u16* __restrict__ wu, u16* __restrict__ hh) {
  constexpr int NSLOT = 3, SLOT = 8192 + 4 * 4096, NK = D_ / 64;
  __shared__ __attribute__((aligned(16))) u16 smem[NSLOT * SLOT];
  const int tid = threadIdx.x, lane = tid & 63, wid = __builtin_amdgcn_readfirstlane(tid >> 6);
  const int xcd = blockIdx.x & 7, idx = blockIdx.x >> 3;
  const int rb = (xcd >> 1) * 4 + idx / 7, cbk = (xcd & 1) * 7 + idx % 7;
  const int m0 = rb * 128, f0 = cbk * 128;
  if (wid >= 8) {
    const int lw = wid - 8;
    const u16* src = ((lw >> 1) ? wu : wg) + f0 + (lw & 1) * 64;
    const u16* asrc = h2 + (size_t)m0 * D_;
    int boff[8], aoff[4];
#pragma unroll
    for (int i = 0; i < 8; ++i) {
      const int sidx = i * 64 + lane, row = sidx >> 3, pos = sidx & 7;
      boff[i] = row * DF_ + ((pos ^ swzB(row)) * 8);
    }
#pragma unroll
    for (int i = 0; i < 4; ++i) {
      const int sidx = (lw * 4 + i) * 64 + lane, row = sidx >> 3, pos = sidx & 7;
      aoff[i] = row * D_ + ((pos ^ swzA(row)) * 8);
    }
    auto issue = [&](int kt) {
      u16* st = smem + (kt % NSLOT) * SLOT;
#pragma unroll
      for (int i = 0; i < 4; ++i) glds16(asrc + kt * 64 + aoff[i], st + (lw * 4 + i) * 512);
#pragma unroll
      for (int i = 0; i < 8; ++i) glds16(src + (size_t)kt * 64 * DF_ + boff[i], st + 8192 + lw * 4096 + i * 512);
    };
    issue(0); issue(1);
    for (int kt = 0; kt < NK; ++kt) {
      if (kt + 1 < NK) wait_vm<12>(); else wait_vm<0>();
      raw_barrier();
      if (kt + 2 < NK) issue(kt + 2);
    }
  } else {
    const int wm = wid >> 2, wn = wid & 3;
    f32x4 accg[4][2], accu[4][2];
#pragma unroll
    for (int a = 0; a < 4; ++a)
#pragma unroll
      for (int b = 0; b < 2; ++b) { accg[a][b] = (f32x4){0.f, 0.f, 0.f, 0.f}; accu[a][b] = (f32x4){0.f, 0.f, 0.f, 0.f}; }
    const int ntg = (wn >> 1) * 4 + (wn & 1) * 2;
    for (int kt = 0; kt < NK; ++kt) {
      raw_barrier();
      const u16* st = smem + (kt % NSLOT) * SLOT;
      mma64<4, 2>(st, st + 8192, accg, wm * 4, ntg, lane);
      mma64<4, 2>(st, st + 8192, accu, wm * 4, 8 + ntg, lane);
    }
#pragma unroll
    for (int mt = 0; mt < 4; ++mt)
#pragma unroll
      for (int nt = 0; nt < 2; ++nt) {
        const int m = wm * 64 + mt * 16 + (lane & 15), n = wn * 32 + nt * 16 + (lane >> 4) * 4;
        float r[4];
#pragma unroll
        for (int j = 0; j < 4; ++j) {
          const float g = rbf(accg[mt][nt][j]), u = rbf(accu[mt][nt][j]);
          const float y = 0.7978845608028654f * (g + 0.044715f * g * g * g);
          const float th = 1.f - 2.f / (__expf(2.f * y) + 1.f);
          const float ge = rbf(0.5f * g * (1.f + th));
          r[j] = ge * u;
        }
        u32x2 w; w[0] = pack2(r[0], r[1]); w[1] = pack2(r[2], r[3]);
        *(u32x2*)(hh + (size_t)(m0 + m) * DF_ + f0 + n) = w;
      }
  }
}

__global__ __launch_bounds__(512) void down_kernel(const u16* __restrict__ hh, const u16* __restrict__ wd,
                                                   const u16* __restrict__ x1, const float* __restrict__ mod,
                                                   u16* __restrict__ out) {
  constexpr int NSLOT = 8;
  __shared__ __attribute__((aligned(16))) u16 smem[NSLOT * 8192];
  const int tid = threadIdx.x, lane = tid & 63, wid = tid >> 6, wm = wid >> 1, wn = wid & 1;
  const int xcd = blockIdx.x & 7, idx = blockIdx.x >> 3;
  const int rb = xcd * 4 + (idx >> 3), cb = idx & 7;
  const int m0 = rb * 64, n0 = cb * 64;
  f32x4 acc[2][2];
#pragma unroll
  for (int a = 0; a < 2; ++a)
#pragma unroll
    for (int b = 0; b < 2; ++b) acc[a][b] = (f32x4){0.f, 0.f, 0.f, 0.f};
  u32x2 xv_pre[2][2]; float4 g_pre[2];
  if (tid < 256) {
#pragma unroll
    for (int mt = 0; mt < 2; ++mt)
#pragma unroll
      for (int nt = 0; nt < 2; ++nt)
        xv_pre[mt][nt] = *(const u32x2*)(x1 + (size_t)(m0 + wm * 32 + mt * 16 + (lane & 15)) * D_ + n0 + wn * 32 + nt * 16 + (lane >> 4) * 4);
#pragma unroll
    for (int nt = 0; nt < 2; ++nt) g_pre[nt] = *(const float4*)(mod + 5 * D_ + n0 + wn * 32 + nt * 16 + (lane >> 4) * 4);
  }
  gemm64_dma<DF_, NSLOT>(hh + (size_t)m0 * DF_, DF_, wd + n0, D_, smem, acc, tid);
  if (tid < 256)
#pragma unroll
  for (int mt = 0; mt < 2; ++mt)
#pragma unroll
    for (int nt = 0; nt < 2; ++nt) {
      const int m = wm * 32 + mt * 16 + (lane & 15), n = wn * 32 + nt * 16 + (lane >> 4) * 4;
      const u32x2 xv = xv_pre[mt][nt];
      const float4 gbv = g_pre[nt];
      u32x2 w;
      w[0] = pack2(lo16(xv[0]) + rbf(gbv.x * rbf(acc[mt][nt][0])), hi16(xv[0]) + rbf(gbv.y * rbf(acc[mt][nt][1])));
      w[1] = pack2(lo16(xv[1]) + rbf(gbv.z * rbf(acc[mt][nt][2])), hi16(xv[1]) + rbf(gbv.w * rbf(acc[mt][nt][3])));
      *(u32x2*)(out + (size_t)(m0 + m) * D_ + n0 + n) = w;
    }
}

extern "C" void kernel_launch(void* const* d_in, const int* in_sizes, int n_in, void* d_out, int out_size,
                              void* d_ws, size_t ws_size, hipStream_t stream) {
  const u16* x = (const u16*)d_in[0];
  const u16* wqkv = (const u16*)d_in[1];
  const u16* wo = (const u16*)d_in[2];
  const u16* wg = (const u16*)d_in[4];
  const u16* wu = (const u16*)d_in[5];
  const u16* wd = (const u16*)d_in[6];
  const u16* c = (const u16*)d_in[7];
  const u16* wmod = (const u16*)d_in[8];
  u16* out = (u16*)d_out;
  char* ws = (char*)d_ws;
  float* mod = (float*)ws;
  float2* stats = (float2*)(ws + 16384);
  u16* xn = (u16*)(ws + 16384 + 131072);
  u16* o = xn + (size_t)S_ * D_;
  u16* x1 = o + (size_t)S_ * D_;
  u16* hh = x1 + (size_t)S_ * D_;

  prep_kernel<<<PREP_MOD_BLOCKS, 256, 0, stream>>>(x, c, wmod, mod, xn);
  ln2_kernel<<<S_ / 4, 256, 0, stream>>>(x, mod, xn, 0, D_);
  qkv_attn_kernel<<<256, 512, 0, stream>>>(xn, wqkv, o);
  wo_kernel<<<256, 512, 0, stream>>>(o, wo, x, mod, x1, stats);
  ln2_kernel<<<S_ / 4, 256, 0, stream>>>(x1, mod, xn, 3 * D_, 4 * D_);
  gateup_kernel<<<16 * 14, 768, 0, stream>>>(xn, wg, wu, hh);
  down_kernel<<<256, 512, 0, stream>>>(hh, wd, x1, mod, out);
}
```
